```python
import torch

def _softmax(x, axis=-1):
    return torch.softmax(x, dim=axis)
def _gelu(x):
    return torch.nn.functional.gelu(x, approximate="tanh")
def _silu(x):
    return torch.nn.functional.silu(x)
def _sigmoid(x):
    return torch.sigmoid(x)
def _layernorm(x, axis=-1, eps=1e-5):
    m = x.mean(dim=axis, keepdim=True)
    v = x.var(dim=axis, keepdim=True, unbiased=False)
    return (x - m) / torch.sqrt(v + eps)
def _scatter_add(tgt, idx, src, axis=0):
    return tgt.clone().index_add_(axis, idx, src)
def _segment_sum(src, seg, n):
    out = torch.zeros((n,) + src.shape[1:], dtype=src.dtype)
    return out.index_add_(0, seg, src)
def _pad(x, pw):
    flat = tuple(v for pair in reversed(pw) for v in pair)
    return torch.nn.functional.pad(x, flat)

kernel_name = 'sequential_preLN__spectral_fnet__dense_activation_relu__prompt'

def setup_inputs():

    _w = (lambda a: (lambda t: t.to(torch.bfloat16) if t.is_floating_point() else t)(torch.as_tensor(a)))
    def _A_setup():
        B, S, D = 1, 2048, 512
        ns = torch.arange(S)[:, None] * torch.arange(S)[None, :]
        nd = torch.arange(D)[:, None] * torch.arange(D)[None, :]
        Ws_re = torch.cos(-2.0 * torch.pi * ns / S).to(float)
        Ws_im = torch.sin(-2.0 * torch.pi * ns / S).to(float)
        Wd_re = torch.cos(-2.0 * torch.pi * nd / D).to(float)
        Wd_im = torch.sin(-2.0 * torch.pi * nd / D).to(float)
        return {
            "x": _w((lambda *s: torch.randn(*s, dtype=torch.bfloat16) * 0.05)(B, S, D)),
            "dft_seq_re": _w(Ws_re),
            "dft_seq_im": _w(Ws_im),
            "dft_hid_re": _w(Wd_re),
            "dft_hid_im": _w(Wd_im),
        }
    _A_in = _A_setup()

    def _B_setup():
        B, S, D, Dff = 1, 2048, 512, 1792
        return {
            "x": _w((lambda *s: torch.randn(*s, dtype=torch.bfloat16) * 0.05)(B, S, D)),
            "w1": _w((lambda *s: torch.randn(*s, dtype=torch.bfloat16) * 0.4)(D, Dff)),
            "w2": _w((lambda *s: torch.randn(*s, dtype=torch.bfloat16) * 0.4)(Dff, D)),
        }
    _B_in = _B_setup()

    return {
        "A_x": _A_in["x"],
        "A_dft_seq_re": _A_in["dft_seq_re"],
        "A_dft_seq_im": _A_in["dft_seq_im"],
        "A_dft_hid_re": _A_in["dft_hid_re"],
        "A_dft_hid_im": _A_in["dft_hid_im"],
        "B_x": _B_in["x"],
        "B_w1": _B_in["w1"],
        "B_w2": _B_in["w2"],
    }

def reference(A_x, A_dft_seq_re, A_dft_seq_im, A_dft_hid_re, A_dft_hid_im, B_x, B_w1, B_w2):
    def _A_ref(x, dft_seq_re, dft_seq_im, dft_hid_re, dft_hid_im):
        a = torch.einsum("bsd,ed->bse", x, dft_hid_re)
        b = torch.einsum("bsd,ed->bse", x, dft_hid_im)
        y = torch.einsum("ts,bse->bte", dft_seq_re, a) - torch.einsum("ts,bse->bte", dft_seq_im, b)
        return y

    def _B_ref(x, w1, w2):
        h = torch.einsum("bsd,df->bsf", x, w1)
        zero = torch.zeros_like(h)
        h = torch.maximum(h, zero)
        return torch.einsum("bsf,fd->bsd", h, w2)

    x = A_x
    h = x + _A_ref(_layernorm(x, axis=-1), A_dft_seq_re, A_dft_seq_im, A_dft_hid_re, A_dft_hid_im)
    h2 = h + _B_ref(_layernorm(h, axis=-1), B_w1, B_w2)
    return h2
```

```cpp
#include <hip/hip_runtime.h>
#include <hip/hip_bf16.h>
#include <stdint.h>

typedef unsigned short u16;
using bf16x8 = __attribute__((ext_vector_type(8))) short;
using f32x4 = __attribute__((ext_vector_type(4))) float;
typedef unsigned v4u __attribute__((ext_vector_type(4)));

constexpr int S = 2048, D = 512, F = 1792;

__device__ __forceinline__ float bf2f(u16 v) { return __uint_as_float(((unsigned)v) << 16); }
__device__ __forceinline__ u16 f2bf(float f) {
  __hip_bfloat16 b = __float2bfloat16(f);
  return *reinterpret_cast<u16*>(&b);
}
__device__ __forceinline__ float rbf(float f) { return bf2f(f2bf(f)); }

__global__ __launch_bounds__(256) void k_ln(const u16* __restrict__ x, u16* __restrict__ y) {
  int row = blockIdx.x * 4 + (threadIdx.x >> 6), lane = threadIdx.x & 63;
  v4u v = *reinterpret_cast<const v4u*>(x + (size_t)row * D + lane * 8);
  float f[8];
  for (int i = 0; i < 4; ++i) { f[2 * i] = __uint_as_float(v[i] << 16); f[2 * i + 1] = __uint_as_float(v[i] & 0xffff0000u); }
  float s = 0; for (int i = 0; i < 8; ++i) s += f[i];
  for (int o = 32; o; o >>= 1) s += __shfl_xor(s, o);
  float m = s * (1.0f / D);
  float q = 0; for (int i = 0; i < 8; ++i) q += (f[i] - m) * (f[i] - m);
  for (int o = 32; o; o >>= 1) q += __shfl_xor(q, o);
  float var = q * (1.0f / D);
  float mb = rbf(m), vb = rbf(var);
  float sd = rbf(sqrtf(rbf(vb + 1e-5f)));
  v4u o;
  for (int i = 0; i < 4; ++i) {
    u16 a = f2bf(rbf(f[2 * i] - mb) / sd), b = f2bf(rbf(f[2 * i + 1] - mb) / sd);
    o[i] = (unsigned)a | ((unsigned)b << 16);
  }
  *reinterpret_cast<v4u*>(y + (size_t)row * D + lane * 8) = o;
}

__global__ __launch_bounds__(256) void k_tr(const u16* __restrict__ in, u16* __restrict__ out, int R, int C) {
  __shared__ u16 t[32][33];
  int bx = blockIdx.x * 32, by = blockIdx.y * 32, tx = threadIdx.x & 31, ty = threadIdx.x >> 5;
  for (int i = ty; i < 32; i += 8) t[i][tx] = in[(size_t)(by + i) * C + bx + tx];
  __syncthreads();
  for (int i = ty; i < 32; i += 8) out[(size_t)(bx + i) * R + by + tx] = t[tx][i];
}

template <int EPI>
__global__ __launch_bounds__(256) void k_gemm(const u16* __restrict__ A0, const u16* __restrict__ A1, int lda, int K0,
                                              const u16* __restrict__ B0, const u16* __restrict__ B1, int ldb, int N0,
                                              int K, const u16* __restrict__ R, u16* __restrict__ C, int ldc) {
  __shared__ __attribute__((aligned(16))) u16 sA[64][72], sB[64][72];
  int bm = blockIdx.y * 64, bn = blockIdx.x * 64;
  int tid = threadIdx.x, wid = tid >> 6, lane = tid & 63, wr = wid >> 1, wc = wid & 1, fr = lane & 15, fq = lane >> 4;
  f32x4 acc[2][2] = {};
  const u16* Bp = bn < N0 ? B0 + (size_t)bn * ldb : B1 + (size_t)(bn - N0) * ldb;
  for (int k0 = 0; k0 < K; k0 += 64) {
    const u16* Ap = k0 < K0 ? A0 + (size_t)bm * lda + k0 : A1 + (size_t)bm * lda + (k0 - K0);
    for (int i = 0; i < 2; ++i) {
      int e = tid + i * 256, r = e >> 3, c = (e & 7) * 8;
      *reinterpret_cast<v4u*>(&sA[r][c]) = *reinterpret_cast<const v4u*>(Ap + (size_t)r * lda + c);
      *reinterpret_cast<v4u*>(&sB[r][c]) = *reinterpret_cast<const v4u*>(Bp + (size_t)r * ldb + k0 + c);
    }
    __syncthreads();
    for (int kk = 0; kk < 2; ++kk) {
      bf16x8 a[2], b[2];
      for (int m = 0; m < 2; ++m) a[m] = *reinterpret_cast<const bf16x8*>(&sA[wr * 32 + m * 16 + fr][kk * 32 + fq * 8]);
      for (int n = 0; n < 2; ++n) b[n] = *reinterpret_cast<const bf16x8*>(&sB[wc * 32 + n * 16 + fr][kk * 32 + fq * 8]);
      for (int m = 0; m < 2; ++m) for (int n = 0; n < 2; ++n)
        acc[m][n] = __builtin_amdgcn_mfma_f32_16x16x32_bf16(a[m], b[n], acc[m][n], 0, 0, 0);
    }
    __syncthreads();
  }
  for (int m = 0; m < 2; ++m) for (int n = 0; n < 2; ++n) for (int j = 0; j < 4; ++j) {
    int r = bm + wr * 32 + m * 16 + fq * 4 + j, c = bn + wc * 32 + n * 16 + fr;
    float v = acc[m][n][j];
    if (EPI == 0) { if (c < 512) C[(size_t)c * 4096 + r] = f2bf(v); else C[(size_t)(c - 512) * 4096 + 2048 + r] = f2bf(-v); }
    else if (EPI == 1) C[(size_t)r * ldc + c] = f2bf(bf2f(R[(size_t)r * ldc + c]) + rbf(v));
    else if (EPI == 2) C[(size_t)r * ldc + c] = f2bf(fmaxf(v, 0.f));
    else C[(size_t)r * ldc + c] = f2bf(bf2f(R[(size_t)r * ldc + c]) + rbf(v));
  }
}

extern "C" void kernel_launch(void* const* d_in, const int* in_sizes, int n_in, void* d_out, int out_size, void* d_ws,
                              size_t ws_size, hipStream_t stream) {
  const u16* x = (const u16*)d_in[0];
  const u16* cr = (const u16*)d_in[1];
  const u16* ci = (const u16*)d_in[2];
  const u16* wr = (const u16*)d_in[3];
  const u16* wi = (const u16*)d_in[4];
  const u16* w1 = (const u16*)d_in[6];
  const u16* w2 = (const u16*)d_in[7];
  u16* out = (u16*)d_out;
  char* ws = (char*)d_ws;
  u16* xn = (u16*)(ws);
  u16* abT = (u16*)(ws + (2 << 20));
  u16* h = (u16*)(ws + (6 << 20));
  u16* hn = (u16*)(ws + (8 << 20));
  u16* u = (u16*)(ws + (10 << 20));
  u16* w1T = (u16*)(ws + (18 << 20));
  u16* w2T = (u16*)(ws + (20 << 20));
  k_ln<<<S / 4, 256, 0, stream>>>(x, xn);
  k_tr<<<dim3(F / 32, D / 32), 256, 0, stream>>>(w1, w1T, D, F);
  k_tr<<<dim3(D / 32, F / 32), 256, 0, stream>>>(w2, w2T, F, D);
  k_gemm<0><<<dim3(1024 / 64, S / 64), 256, 0, stream>>>(xn, xn, D, D, wr, wi, D, 512, D, nullptr, abT, 0);
  k_gemm<1><<<dim3(D / 64, S / 64), 256, 0, stream>>>(cr, ci, S, S, abT, abT, 4096, 512, 4096, x, h, D);
  k_ln<<<S / 4, 256, 0, stream>>>(h, hn);
  k_gemm<2><<<dim3(F / 64, S / 64), 256, 0, stream>>>(hn, hn, D, D, w1T, w1T, D, F, D, nullptr, u, F);
  k_gemm<3><<<dim3(D / 64, S / 64), 256, 0, stream>>>(u, u, F, F, w2T, w2T, F, D, F, h, out, D);
}
```

```cpp
#include <hip/hip_runtime.h>
#include <hip/hip_bf16.h>
#include <stdint.h>

#define DI __device__ __forceinline__
#define GAS __attribute__((address_space(1)))
#define LAS __attribute__((address_space(3)))
typedef unsigned short u16;
typedef unsigned long long u64;
typedef unsigned v4u __attribute__((ext_vector_type(4)));
typedef unsigned v2u __attribute__((ext_vector_type(2)));
typedef float f32x16 __attribute__((ext_vector_type(16)));
typedef float f32x4 __attribute__((ext_vector_type(4)));
typedef float f32x2 __attribute__((ext_vector_type(2)));
typedef __bf16 bf16x2 __attribute__((ext_vector_type(2)));
typedef short v4i16 __attribute__((ext_vector_type(4)));
typedef GAS u64 gu64;
typedef __amdgpu_buffer_rsrc_t rsrc_t;
#define RLX_AGENT __ATOMIC_RELAXED, __HIP_MEMORY_SCOPE_AGENT

#define MK_STAMP 0

constexpr int SEQ = 2048, DM = 512, FF = 1792;
constexpr unsigned WS_EPOCH = 0;
constexpr unsigned WS_FLAG = 4096;
constexpr unsigned FL_AB = 0;
constexpr unsigned FL_W1 = 1024;
constexpr unsigned FL_W2 = 1536;
constexpr unsigned FL_EX = 2048;
constexpr unsigned FL_H = 6144;
constexpr unsigned FL_U = 7168;
constexpr unsigned FL_END = 8192;
constexpr unsigned WS_AB = 2u << 20;
constexpr unsigned WS_EX = 6u << 20;
constexpr unsigned WS_H = 14u << 20;
constexpr unsigned WS_HS = 16u << 20;
constexpr unsigned WS_CS = WS_HS + (512u << 10);
constexpr unsigned WS_U = 17u << 20;
constexpr unsigned WS_W1T = 24u << 20;
constexpr unsigned WS_W2T = 26u << 20;
constexpr unsigned WS_STAMP = 28u << 20;
constexpr unsigned WS_BYTES = 30u << 20;
static_assert(WS_FLAG + FL_END * 128 <= WS_AB, "flags");

constexpr int LDS_BYTES = 160 * 1024;
constexpr int L_MISC = 144 * 1024;
constexpr int P1_STAGE = 24 * 1024;
constexpr int P1_NST = 3;
constexpr int P1_BASE = 72 * 1024;
constexpr int C_NSLOT = 9;

constexpr unsigned SPIN_CAP = 20000;

template <int... I> struct iseq {};
template <int N, int... I> struct mkseq : mkseq<N - 1, N - 1, I...> {};
template <int... I> struct mkseq<0, I...> { typedef iseq<I...> type; };
template <int I> struct ic { static constexpr int v = I; };
template <class F, int... I> DI void sfor_(F&& f, iseq<I...>) { (f(ic<I>{}), ...); }
template <int N, class F> DI void sfor(F&& f) { sfor_(f, typename mkseq<N>::type{}); }

DI unsigned rfl(unsigned x) { return __builtin_amdgcn_readfirstlane(x); }
DI rsrc_t mk_rsrc(const void* p, unsigned bytes) { return __builtin_amdgcn_make_buffer_rsrc((void*)p, 0, bytes, 0x00020000); }
DI unsigned cvtpk(float lo, float hi) { f32x2 v = {lo, hi}; bf16x2 b = __builtin_convertvector(v, bf16x2); return __builtin_bit_cast(unsigned, b); }
DI float bflo(unsigned v) { return __uint_as_float(v << 16); }
DI float bfhi(unsigned v) { return __uint_as_float(v & 0xffff0000u); }
DI float rbf(float f) { return bflo(cvtpk(f, f)); }
DI float dot2(unsigned a, unsigned b, float c) {
  return __builtin_amdgcn_fdot2_f32_bf16(__builtin_bit_cast(bf16x2, a), __builtin_bit_cast(bf16x2, b), c, false);
}

#define WAIT_VM(n) asm volatile("s_waitcnt vmcnt(%0)" ::"n"(n) : "memory")
#define BARRIER() asm volatile("s_barrier" ::: "memory")
#define WAIT_LGKM0() asm volatile("s_waitcnt lgkmcnt(0)" ::: "memory")

DI void glds(rsrc_t rs, unsigned voff, unsigned soff, unsigned lds) {
  asm volatile("s_mov_b32 m0, %0\n\ts_nop 0\n\tbuffer_load_dwordx4 %1, %2, %3 offen lds" ::"s"(lds), "v"(voff), "s"(rs), "s"(soff) : "memory");
}
DI void glds_sc1(rsrc_t rs, unsigned voff, unsigned soff, unsigned lds) {
  asm volatile("s_mov_b32 m0, %0\n\ts_nop 0\n\tbuffer_load_dwordx4 %1, %2, %3 offen sc1 lds" ::"s"(lds), "v"(voff), "s"(rs), "s"(soff) : "memory");
}
template <int SL, int IMM> DI void ld_a(rsrc_t rs, unsigned voff, unsigned soff) {
  asm volatile("buffer_load_dwordx4 a[%c0:%c1], %2, %3, %4 offen offset:%c5 sc1" ::"i"(4 * SL), "i"(4 * SL + 3), "v"(voff), "s"(rs), "s"(soff), "i"(IMM) : "memory");
}
template <int IMM> DI void ld_v(v4u& d, rsrc_t rs, unsigned voff, unsigned soff) {
  asm volatile("buffer_load_dwordx4 %0, %1, %2, %3 offen offset:%c4 sc1" : "=v"(d) : "v"(voff), "s"(rs), "s"(soff), "i"(IMM) : "memory");
}
template <int OFF> DI void ds_rd(v4u& d, unsigned addr) { asm volatile("ds_read_b128 %0, %1 offset:%c2" : "=v"(d) : "v"(addr), "i"(OFF) : "memory"); }
template <int N> DI void wait_lgkm(v4u& a) { asm volatile("s_waitcnt lgkmcnt(%c1)" : "+v"(a) : "i"(N)); }
template <int N> DI void wait_vm(v4u& a) { asm volatile("s_waitcnt vmcnt(%c1)" : "+v"(a) : "i"(N)); }
template <int SL, bool Z> DI void mfma_a(f32x16& acc, const v4u& b) {
  if constexpr (Z) asm volatile("v_mfma_f32_32x32x16_bf16 %0, a[%c2:%c3], %1, 0" : "=&v"(acc) : "v"(b), "i"(4 * SL), "i"(4 * SL + 3));
  else asm volatile("v_mfma_f32_32x32x16_bf16 %0, a[%c2:%c3], %1, %0" : "+v"(acc) : "v"(b), "i"(4 * SL), "i"(4 * SL + 3));
}
template <bool Z> DI void mfma_v(f32x16& acc, const v4u& a, const v4u& b) {
  if constexpr (Z) asm volatile("v_mfma_f32_32x32x16_bf16 %0, %1, %2, 0" : "=&v"(acc) : "v"(a), "v"(b));
  else asm volatile("v_mfma_f32_32x32x16_bf16 %0, %1, %2, %0" : "+v"(acc) : "v"(a), "v"(b));
}
DI void mfma_done(f32x16& a) { asm volatile("s_nop 15\n\ts_nop 3" : "+v"(a)); }
DI void pin(f32x16& a) { asm volatile("" : "+v"(a)); }
DI void st_sc1(rsrc_t rs, v4u v, unsigned voff, unsigned soff) { __builtin_amdgcn_raw_buffer_store_b128(v, rs, voff, soff, 16); }

DI void set_flag(char* ws, unsigned idx, u64 tag, int lane) {
  if (lane == 0) __hip_atomic_store((gu64*)(ws + WS_FLAG + (size_t)idx * 128), tag, RLX_AGENT);
}
DI void wait_flags(char* ws, unsigned base, int n, u64 tag, int lane) {
  gu64* p = (gu64*)(ws + WS_FLAG + (size_t)(base + (lane < n ? lane : n - 1)) * 128);
#pragma unroll 1
  for (unsigned s = 0; s < SPIN_CAP; ++s) {
    u64 v = __hip_atomic_load(p, RLX_AGENT);
    if (__all(v == tag)) break;
  }
  asm volatile("" ::: "memory");
}
DI v4u pack_lo(const f32x16& a) { v4u r = {cvtpk(a[0], a[1]), cvtpk(a[2], a[3]), cvtpk(a[4], a[5]), cvtpk(a[6], a[7])}; return r; }
DI v4u pack_hi(const f32x16& a) { v4u r = {cvtpk(a[8], a[9]), cvtpk(a[10], a[11]), cvtpk(a[12], a[13]), cvtpk(a[14], a[15])}; return r; }
DI int swap23(int r) { return (r & ~12) | ((r & 4) << 1) | ((r & 8) >> 1); }
DI void ln_scale(float s, float q, float& rs, float& mrs) {
  float m = s * (1.0f / DM);
  float var = fmaxf(q * (1.0f / DM) - m * m, 0.f);
  float sd = rbf(sqrtf(rbf(rbf(var) + 1e-5f)));
  rs = 1.0f / sd;
  mrs = rbf(m) * rs;
}

#define STAMP(i) do {} while (0)

__global__ __launch_bounds__(256, 1) void fnet_mega(const u16* __restrict__ x, const u16* __restrict__ cr, const u16* __restrict__ ci,
                                                   const u16* __restrict__ wr, const u16* __restrict__ wi, const u16* __restrict__ w1,
                                                   const u16* __restrict__ w2, u16* __restrict__ out, char* ws) {
  extern __shared__ __attribute__((aligned(16))) char smem[];
  const int b = blockIdx.x, xcd = b & 7, idx = b >> 3;
  const int tid = threadIdx.x, lane = tid & 63, w = rfl(tid >> 6);
  const unsigned lds0 = (unsigned)(uintptr_t)(LAS char*)smem;
  asm volatile("" ::: "a255");
  STAMP(0);
  const u64 ep = __hip_atomic_load((gu64*)(ws + WS_EPOCH), RLX_AGENT);
  const rsrc_t rws = mk_rsrc(ws, WS_BYTES);
  const int r32 = lane & 31, hh = lane >> 5;
  const unsigned lane16 = lane * 16;

  const int sp = (xcd >> 1) * 8 + (idx >> 2), ch = xcd & 1, jt = idx & 3;
  {
    const rsrc_t rx = mk_rsrc(x + (size_t)sp * 64 * DM, 64 * 1024);
    const rsrc_t rw = mk_rsrc((ch ? wi : wr) + (size_t)jt * 128 * DM, 128 * 1024);
    const int rot = (2 * jt + (idx >> 2)) & 7;
    unsigned vx[2], vw[4];
#pragma unroll
    for (int p = 0; p < 2; ++p) { int rho = (2 * w + p) * 8 + (lane >> 3); vx[p] = swap23(rho) * 1024 + (((lane & 7) ^ ((rho >> 1) & 7)) << 4); }
#pragma unroll
    for (int p = 0; p < 4; ++p) { int rho = (4 * w + p) * 8 + (lane >> 3); vw[p] = swap23(rho) * 1024 + (((lane & 7) ^ ((rho >> 1) & 7)) << 4); }
    auto stage = [&](int st) {
      unsigned so = ((st + rot) & 7) * 128, base = lds0 + P1_BASE + (st % P1_NST) * P1_STAGE;
#pragma unroll
      for (int p = 0; p < 2; ++p) glds(rx, vx[p], so, base + (2 * w + p) * 1024);
#pragma unroll
      for (int p = 0; p < 4; ++p) glds(rw, vw[p], so, base + 8192 + (4 * w + p) * 1024);
    };
    stage(0); stage(1);
    unsigned ra[4], rb[4];
#pragma unroll
    for (int k = 0; k < 4; ++k) { ra[k] = lds0 + P1_BASE + r32 * 128 + (((2 * k + hh) ^ ((r32 >> 1) & 7)) << 4); rb[k] = ra[k] + 8192 + w * 4096; }
    const unsigned rs_addr = lds0 + P1_BASE + (tid >> 2) * 128 + (tid & 3) * 16;
    f32x16 acc0, acc1, accw;
    v4u ones = {0x3f803f80u, 0x3f803f80u, 0x3f803f80u, 0x3f803f80u};
    asm volatile("" : "+v"(ones));
    float ssum = 0.f, ssq = 0.f;
    sfor<8>([&](auto ST) {
      constexpr int st = ST.v, so = (st % P1_NST) * P1_STAGE;
      if constexpr (st < 7) WAIT_VM(6); else WAIT_VM(0);
      BARRIER();
      v4u a0[4], a1[4], bb[4], s0, s1;
      sfor<4>([&](auto K) { constexpr int k = K.v; ds_rd<so>(a0[k], ra[k]); ds_rd<so>(bb[k], rb[k]); ds_rd<so + 4096>(a1[k], ra[k]); });
      ds_rd<so>(s0, rs_addr); ds_rd<so + 64>(s1, rs_addr);
      if constexpr (st + 2 < 8) stage(st + 2);
      sfor<4>([&](auto K) {
        constexpr int k = K.v; constexpr bool Z = (st == 0 && k == 0);
        wait_lgkm<12 - 3 * k>(a0[k]); asm volatile("" : "+v"(bb[k]));
        mfma_v<Z>(acc0, a0[k], bb[k]);
        mfma_v<Z>(accw, ones, bb[k]);
        wait_lgkm<11 - 3 * k>(a1[k]);
        mfma_v<Z>(acc1, a1[k], bb[k]);
      });
      wait_lgkm<0>(s0); asm volatile("" : "+v"(s1));
#pragma unroll
      for (int e = 0; e < 4; ++e) { ssum = dot2(s0[e], 0x3f803f80u, ssum); ssq = dot2(s0[e], s0[e], ssq); ssum = dot2(s1[e], 0x3f803f80u, ssum); ssq = dot2(s1[e], s1[e], ssq); }
    });
    STAMP(1);
    ssum += __shfl_xor(ssum, 1); ssq += __shfl_xor(ssq, 1);
    ssum += __shfl_xor(ssum, 2); ssq += __shfl_xor(ssq, 2);
    float rs, mrs; ln_scale(ssum, ssq, rs, mrs);
    if (ch) { rs = -rs; mrs = -mrs; }
    float* stat = (float*)(smem + L_MISC);
    if ((tid & 3) == 0) { stat[tid >> 2] = rs; stat[64 + (tid >> 2)] = mrs; }
    WAIT_LGKM0(); BARRIER();
    mfma_done(acc0); pin(acc1); pin(accw);
    const float wsum = accw[0];
    const unsigned abo = WS_AB + ((((ch * 2 + (sp >> 4)) * 4 + jt) * 16 + (sp & 15)) * 4 + w) * 4096;
    auto fin = [&](f32x16& acc, int mt) {
#pragma unroll
      for (int g = 0; g < 4; ++g) {
        f32x4 r4 = *(const f32x4*)(stat + mt * 32 + 8 * g + 4 * hh), m4 = *(const f32x4*)(stat + 64 + mt * 32 + 8 * g + 4 * hh);
#pragma unroll
        for (int e = 0; e < 4; ++e) acc[4 * g + e] = acc[4 * g + e] * r4[e] - m4[e] * wsum;
      }
      st_sc1(rws, pack_lo(acc), lane16 + mt * 2048, abo);
      st_sc1(rws, pack_hi(acc), lane16 + mt * 2048 + 1024, abo);
    };
    fin(acc0, 0); fin(acc1, 1);
  }
  const u64 tag = ep * 6364136223846793005ull + 1442695040888963407ull;
  STAMP(2);
  WAIT_LGKM0(); BARRIER();

  const int q = xcd >> 1, pi = (xcd & 1) * 8 + (idx >> 2), pj = idx & 3;
  const rsrc_t rc = mk_rsrc((q >> 1 ? ci : cr) + (size_t)pi * 128 * SEQ + (q & 1) * 1024, 128 * 4096 - (q & 1) * 2048);
  const int rot2 = (4 * pj + 2 * (idx >> 2)) & 15;
  unsigned vc[4];
#pragma unroll
  for (int p = 0; p < 4; ++p) { int rho = p * 8 + (lane >> 3); vc[p] = ((w ^ q) * 32 + rho) * 4096 + (((lane & 7) ^ ((rho >> 1) & 7)) << 4); }
  auto stage_c = [&](int st) {
    unsigned so = ((st + rot2) & 15) * 128, base = lds0 + (st % C_NSLOT) * 16384 + w * 4096;
#pragma unroll
    for (int p = 0; p < 4; ++p) glds(rc, vc[p], so, base + p * 1024);
  };
  const int unit = b * 4 + w;
  const bool has_unit = unit < 896, is_w2 = unit >= 448;
  const int un = is_w2 ? unit - 448 : unit;
  const int nb = is_w2 ? un / 56 : un >> 4, kb = is_w2 ? un % 56 : un & 15;
  if (has_unit) {
    const unsigned ld = is_w2 ? DM * 2 : FF * 2;
    const rsrc_t rwt = mk_rsrc(is_w2 ? w2 : w1, DM * FF * 2);
    const unsigned vo = (lane >> 3) * ld + (lane & 7) * 16;
#pragma unroll
    for (int p = 0; p < 4; ++p) glds(rwt, vo, (kb * 32 + p * 8) * ld + nb * 128, lds0 + L_MISC + w * 4096 + p * 1024);
    WAIT_VM(4);
  } else WAIT_VM(0);
  set_flag(ws, FL_AB + (((ch * 2 + (sp >> 4)) * 4 + jt) * 4 + w) * 16 + (sp & 15), tag, lane);
  STAMP(3);
  stage_c(0); stage_c(1);
  if (has_unit) {
    WAIT_VM(9);
    const int G = lane >> 4, qq = (lane >> 2) & 3, pp = lane & 3;
    const LAS char* tb = (const LAS char*)smem + L_MISC + w * 4096 + (8 * (G >> 1) + qq) * 128 + (16 * (G & 1) + 8 * (pp & 1) + 4 * (pp >> 1)) * 2;
    float cs[2] = {0.f, 0.f};
#pragma unroll
    for (int ks = 0; ks < 2; ++ks)
#pragma unroll
      for (int nt = 0; nt < 2; ++nt) {
        v4i16 lo = __builtin_amdgcn_ds_read_tr16_b64_v4i16((LAS v4i16*)(tb + ks * 2048 + nt * 64));
        v4i16 hi = __builtin_amdgcn_ds_read_tr16_b64_v4i16((LAS v4i16*)(tb + ks * 2048 + 512 + nt * 64));
        v2u l2 = __builtin_bit_cast(v2u, lo), h2 = __builtin_bit_cast(v2u, hi);
        v4u f = {l2[0], l2[1], h2[0], h2[1]};
        unsigned so = is_w2 ? WS_W2T + ((nb * 112 + kb * 2 + ks) * 2 + nt) * 1024 : WS_W1T + ((nb * 2 + nt) * 32 + kb * 2 + ks) * 1024;
        st_sc1(rws, f, lane16, so);
#pragma unroll
        for (int e = 0; e < 4; ++e) cs[nt] = dot2(f[e], 0x3f803f80u, cs[nt]);
      }
    if (!is_w2) {
      cs[0] += __shfl_xor(cs[0], 32); cs[1] += __shfl_xor(cs[1], 32);
      float v = hh ? cs[1] : cs[0];
      __builtin_amdgcn_raw_buffer_store_b32(__float_as_uint(v), rws, (hh * 32 + swap23(r32)) * 4, WS_CS + (kb * FF + nb * 64) * 4, 16);
    }
  }
  stage_c(2); stage_c(3);
  if (has_unit) { WAIT_VM(8); set_flag(ws, (is_w2 ? FL_W2 : FL_W1) + un, tag, lane); }
  v4u xr0, xr1;
  { const u16* xp = x + (size_t)(pi * 128 + q * 32 + r32) * DM + pj * 128 + w * 32 + hh * 8; xr0 = *(const v4u*)xp; xr1 = *(const v4u*)(xp + 16); }
  STAMP(4);
  wait_flags(ws, FL_AB + ((q * 4 + pj) * 4 + w) * 16, 16, tag, lane);
  STAMP(5);

  f32x16 acc[4];
  {
    const unsigned abo = WS_AB + (q * 4 + pj) * 16 * 16384 + w * 4096;
    auto load_ab = [&](auto ST) {
      constexpr int st = ST.v;
      unsigned so = abo + ((st + rot2) & 15) * 16384;
      ld_a<4 * st + 0, 0>(rws, lane16, so); ld_a<4 * st + 1, 1024>(rws, lane16, so); ld_a<4 * st + 2, 2048>(rws, lane16, so); ld_a<4 * st + 3, 3072>(rws, lane16, so);
    };
    load_ab(ic<0>{}); load_ab(ic<1>{}); load_ab(ic<2>{});
    BARRIER();
    unsigned cb[3][4];
#pragma unroll
    for (int k = 0; k < 4; ++k) { cb[0][k] = lds0 + r32 * 128 + (((2 * k + hh) ^ ((r32 >> 1) & 7)) << 4); cb[1][k] = cb[0][k] + 65536; cb[2][k] = cb[0][k] + 131072; }
    v4u bf[2][4];
    sfor<4>([&](auto T) { ds_rd<T.v * 4096>(bf[0][T.v], cb[0][0]); });
    sfor<16>([&](auto ST) {
      constexpr int st = ST.v;
      constexpr int n_after = st == 0 ? 8 : st == 1 ? 4 + 8 : st == 2 ? 16 : ((st + 1 <= 15 ? 4 : 0) + ((st + 1 <= 15 ? 4 : 0) + (st + 2 <= 15 ? 4 : 0)) + ((st + 2 <= 15 ? 4 : 0) + (st + 3 <= 15 ? 4 : 0)));
      WAIT_VM(n_after);
      sfor<4>([&](auto K) {
        constexpr int k = K.v, cur = k & 1, nxt = cur ^ 1;
        if constexpr (k == 0 && st + 3 < 16) load_ab(ic<(st + 3 < 16 ? st + 3 : 0)>{});
        if constexpr (k == 3 && st + 1 < 16) {
          if constexpr (st + 1 >= 4) {
            constexpr int na = ((st + 1 <= 15 ? 4 : 0) + (st + 2 <= 15 ? 4 : 0)) + ((st + 2 <= 15 ? 4 : 0) + (st + 3 <= 15 ? 4 : 0)) + (st + 3 <= 15 ? 4 : 0);
            WAIT_VM(na);
          }
          BARRIER();
        }
        sfor<4>([&](auto T) {
          constexpr int t = T.v;
          constexpr bool more = !(st == 15 && k == 3);
          wait_lgkm<more ? 3 : 3 - t>(bf[cur][t]);
          mfma_a<4 * st + k, st == 0 && k == 0>(acc[t], bf[cur][t]);
          if constexpr (more) {
            constexpr int nst = k == 3 ? st + 1 : st, nk = (k + 1) & 3, sl = nst % C_NSLOT;
            ds_rd<(sl & 3) * 16384 + t * 4096>(bf[nxt][t], cb[sl >> 2][nk]);
          }
        });
        if constexpr (k == 3 && st + 4 < 16) stage_c(st + 4);
      });
    });
  }
  STAMP(6);

  const int cl = xcd * 4 + (idx >> 3), km = idx & 7;
  const bool ffn1 = km < 7;
  {
    mfma_done(acc[3]); pin(acc[0]); pin(acc[1]); pin(acc[2]);
    const unsigned exb = WS_EX + (pi * 4 + pj) * 4 * (4 * 4 * 2048);
#pragma unroll
    for (int t = 1; t < 4; ++t) {
      unsigned so = exb + (((q ^ t) * 4 + t) * 4 + w) * 2048;
      st_sc1(rws, pack_lo(acc[t]), lane16, so);
      st_sc1(rws, pack_hi(acc[t]), lane16 + 1024, so);
    }
    WAIT_VM(0);
    if (lane < 3) __hip_atomic_store((gu64*)(ws + WS_FLAG + (size_t)(FL_EX + (((pi * 4 + pj) * 4 + (q ^ (lane + 1))) * 4 + w) * 4 + lane) * 128), tag, RLX_AGENT);
    STAMP(7);
    const unsigned w1o = WS_W1T + (km * 4 + w) * 65536;
    float csum = 0.f;
    if (ffn1) {
      wait_flags(ws, FL_W1 + (km * 4 + w) * 16, 16, tag, lane);
      sfor<8>([&](auto G) { constexpr int g = G.v; unsigned so = w1o + (g >> 2) * 32768 + (g & 3) * 4096;
        ld_a<(g >> 2) * 32 + (g & 3) * 4 + 0, 0>(rws, lane16, so); ld_a<(g >> 2) * 32 + (g & 3) * 4 + 1, 1024>(rws, lane16, so);
        ld_a<(g >> 2) * 32 + (g & 3) * 4 + 2, 2048>(rws, lane16, so); ld_a<(g >> 2) * 32 + (g & 3) * 4 + 3, 3072>(rws, lane16, so); });
    }
    wait_flags(ws, FL_EX + (((pi * 4 + pj) * 4 + q) * 4 + w) * 4, 3, tag, lane);
    STAMP(8);
    v4u e[6];
#pragma unroll
    for (int t = 1; t < 4; ++t) {
      unsigned so = exb + ((q * 4 + t) * 4 + w) * 2048;
      e[2 * t - 2] = __builtin_amdgcn_raw_buffer_load_b128(rws, lane16, so, 16);
      e[2 * t - 1] = __builtin_amdgcn_raw_buffer_load_b128(rws, lane16 + 1024, so, 16);
    }
    f32x16 hv = acc[0];
#pragma unroll
    for (int d = 0; d < 4; ++d) {
      hv[2 * d] += bflo(e[0][d]) + bflo(e[2][d]) + bflo(e[4][d]); hv[2 * d + 1] += bfhi(e[0][d]) + bfhi(e[2][d]) + bfhi(e[4][d]);
      hv[8 + 2 * d] += bflo(e[1][d]) + bflo(e[3][d]) + bflo(e[5][d]); hv[8 + 2 * d + 1] += bfhi(e[1][d]) + bfhi(e[3][d]) + bfhi(e[5][d]);
      hv[2 * d] += bflo(xr0[d]); hv[2 * d + 1] += bfhi(xr0[d]); hv[8 + 2 * d] += bflo(xr1[d]); hv[8 + 2 * d + 1] += bfhi(xr1[d]);
    }
    v4u h0 = pack_lo(hv), h1 = pack_hi(hv);
    const int tt = pi * 4 + q, part = pj * 4 + w;
    st_sc1(rws, h0, lane16, WS_H + (tt * 32 + part * 2) * 1024);
    st_sc1(rws, h1, lane16 + 1024, WS_H + (tt * 32 + part * 2) * 1024);
    float s = 0.f, sq = 0.f;
#pragma unroll
    for (int d = 0; d < 4; ++d) { s = dot2(h0[d], 0x3f803f80u, s); sq = dot2(h0[d], h0[d], sq); s = dot2(h1[d], 0x3f803f80u, s); sq = dot2(h1[d], h1[d], sq); }
    s += __shfl_xor(s, 32); sq += __shfl_xor(sq, 32);
    __builtin_amdgcn_raw_buffer_store_b32(__float_as_uint(hh ? sq : s), rws, r32 * 8 + hh * 4, WS_HS + (tt * 16 + part) * 256, 16);
    WAIT_VM(0);
    set_flag(ws, FL_H + tt * 16 + part, tag, lane);
    STAMP(9);
    if (ffn1) {
      sfor<8>([&](auto G) { constexpr int g = G.v; unsigned so = w1o + (g >> 2) * 32768 + 16384 + (g & 3) * 4096;
        ld_a<(g >> 2) * 32 + 16 + (g & 3) * 4 + 0, 0>(rws, lane16, so); ld_a<(g >> 2) * 32 + 16 + (g & 3) * 4 + 1, 1024>(rws, lane16, so);
        ld_a<(g >> 2) * 32 + 16 + (g & 3) * 4 + 2, 2048>(rws, lane16, so); ld_a<(g >> 2) * 32 + 16 + (g & 3) * 4 + 3, 3072>(rws, lane16, so); });
#pragma unroll
      for (int k = 0; k < 16; ++k) csum += __uint_as_float(__builtin_amdgcn_raw_buffer_load_b32(rws, lane * 4, WS_CS + (k * FF + (km * 4 + w) * 64) * 4, 16));
      ((float*)(smem + L_MISC))[w * 64 + lane] = csum;
    }
  }

  {
    wait_flags(ws, FL_H + (cl * 2 + (w >> 1)) * 16 + (w & 1) * 8, 8, tag, lane);
    BARRIER();
    STAMP(10);
#pragma unroll
    for (int p = 0; p < 16; ++p) glds_sc1(rws, lane16, WS_H + (cl * 64 + w * 16 + p) * 1024, lds0 + (w * 16 + p) * 1024);
#pragma unroll
    for (int p = 0; p < 2; ++p) glds_sc1(rws, lane16, WS_HS + (cl * 8 + w * 2 + p) * 1024, lds0 + 65536 + (w * 2 + p) * 1024);
    WAIT_VM(0); WAIT_LGKM0();
    BARRIER();
    STAMP(11);
    if (ffn1) {
      const unsigned hb = lds0 + lane16;
      v4u bf[2][2];
      ds_rd<0>(bf[0][0], hb); ds_rd<32768>(bf[0][1], hb);
      sfor<32>([&](auto K) {
        constexpr int k = K.v, cur = k & 1, nxt = cur ^ 1;
        if constexpr (k + 1 < 32) { ds_rd<(k + 1) * 1024>(bf[nxt][0], hb); ds_rd<32768 + (k + 1) * 1024>(bf[nxt][1], hb); }
        wait_lgkm<(k + 1 < 32) ? 3 : 1>(bf[cur][0]);
        mfma_a<k, k == 0>(acc[0], bf[cur][0]);
        mfma_a<32 + k, k == 0>(acc[1], bf[cur][0]);
        wait_lgkm<(k + 1 < 32) ? 2 : 0>(bf[cur][1]);
        mfma_a<k, k == 0>(acc[2], bf[cur][1]);
        mfma_a<32 + k, k == 0>(acc[3], bf[cur][1]);
      });
      float rs[2], mrs[2];
#pragma unroll
      for (int t = 0; t < 2; ++t) {
        float s = 0.f, sq = 0.f;
#pragma unroll
        for (int p = 0; p < 8; ++p) { f32x2 v = *(const f32x2*)(smem + 65536 + (t * 16 + hh * 8 + p) * 256 + r32 * 8); s += v[0]; sq += v[1]; }
        s += __shfl_xor(s, 32); sq += __shfl_xor(sq, 32);
        ln_scale(s, sq, rs[t], mrs[t]);
      }
      mfma_done(acc[3]); pin(acc[0]); pin(acc[1]); pin(acc[2]);
      STAMP(12);
      const float* csl = (const float*)(smem + L_MISC) + w * 64 + hh * 8;
#pragma unroll
      for (int f = 0; f < 2; ++f) {
        f32x4 c0 = *(const f32x4*)(csl + f * 32), c1 = *(const f32x4*)(csl + f * 32 + 4), c2 = *(const f32x4*)(csl + f * 32 + 16), c3 = *(const f32x4*)(csl + f * 32 + 20);
#pragma unroll
        for (int t = 0; t < 2; ++t) {
          f32x16& a = acc[t * 2 + f];
#pragma unroll
          for (int e = 0; e < 4; ++e) {
            a[e] = fmaxf(a[e] * rs[t] - mrs[t] * c0[e], 0.f); a[4 + e] = fmaxf(a[4 + e] * rs[t] - mrs[t] * c1[e], 0.f);
            a[8 + e] = fmaxf(a[8 + e] * rs[t] - mrs[t] * c2[e], 0.f); a[12 + e] = fmaxf(a[12 + e] * rs[t] - mrs[t] * c3[e], 0.f);
          }
          unsigned so = WS_U + ((cl * 2 + t) * 112 + ((km * 4 + w) * 2 + f) * 2) * 1024;
          st_sc1(rws, pack_lo(a), lane16, so);
          st_sc1(rws, pack_hi(a), lane16 + 1024, so);
        }
      }
      WAIT_VM(0);
      set_flag(ws, FL_U + cl * 32 + km * 4 + w, tag, lane);
    }
    STAMP(13);
  }

  {
    wait_flags(ws, FL_W2 + km * 56 + w * 14, 14, tag, lane);
    const unsigned w2o = WS_W2T + (km * 112 + w * 28) * 2048;
    const unsigned va0 = lane16 + (w & 1) * 1024, va1 = lane16 + ((w & 1) ^ 1) * 1024;
    sfor<14>([&](auto G) { constexpr int g = G.v; unsigned so = w2o + g * 4096;
      ld_a<4 * g + 0, 0>(rws, va0, so); ld_a<4 * g + 1, 0>(rws, va1, so); ld_a<4 * g + 2, 2048>(rws, va0, so); ld_a<4 * g + 3, 2048>(rws, va1, so); });
    wait_flags(ws, FL_U + cl * 32 + w * 7, 7, tag, lane);
    STAMP(14);
    const unsigned u0 = WS_U + ((cl * 2 + (w >> 1)) * 112 + w * 28) * 1024, u1 = WS_U + ((cl * 2 + ((w >> 1) ^ 1)) * 112 + w * 28) * 1024;
    constexpr int UD = 8;
    v4u ub[UD][2];
    auto ldu = [&](auto K) { constexpr int k = K.v; ld_v<(k & 3) * 1024>(ub[k % UD][0], rws, lane16, u0 + (k >> 2) * 4096); ld_v<(k & 3) * 1024>(ub[k % UD][1], rws, lane16, u1 + (k >> 2) * 4096); };
    sfor<UD>([&](auto K) { ldu(K); });
    sfor<28>([&](auto K) {
      constexpr int k = K.v, last = (k + UD - 1 < 27 ? k + UD - 1 : 27);
      wait_vm<2 * (last - k) + 1>(ub[k % UD][0]);
      mfma_a<2 * k, k == 0>(acc[0], ub[k % UD][0]);
      mfma_a<2 * k + 1, k == 0>(acc[1], ub[k % UD][0]);
      wait_vm<2 * (last - k)>(ub[k % UD][1]);
      mfma_a<2 * k, k == 0>(acc[2], ub[k % UD][1]);
      mfma_a<2 * k + 1, k == 0>(acc[3], ub[k % UD][1]);
      if constexpr (k + UD < 28) ldu(ic<(k + UD < 28 ? k + UD : 0)>{});
    });
    mfma_done(acc[3]); pin(acc[0]); pin(acc[1]); pin(acc[2]);
    STAMP(15);
    char* red = smem + 73728;
#pragma unroll
    for (int t = 1; t < 4; ++t)
#pragma unroll
      for (int g = 0; g < 4; ++g) {
        f32x4 v = {acc[t][4 * g], acc[t][4 * g + 1], acc[t][4 * g + 2], acc[t][4 * g + 3]};
        *(f32x4*)(red + (((w ^ t) * 3 + (t - 1)) * 4 + g) * 1024 + lane16) = v;
      }
    WAIT_LGKM0(); BARRIER();
    f32x16 o = acc[0];
#pragma unroll
    for (int t = 0; t < 3; ++t)
#pragma unroll
      for (int g = 0; g < 4; ++g) {
        f32x4 v = *(const f32x4*)(red + ((w * 3 + t) * 4 + g) * 1024 + lane16);
#pragma unroll
        for (int e = 0; e < 4; ++e) o[4 * g + e] += v[e];
      }
    const int dt = w & 1, tt = w >> 1;
    v4u h0 = *(const v4u*)(smem + (tt * 32 + km * 4 + dt * 2) * 1024 + lane16), h1 = *(const v4u*)(smem + (tt * 32 + km * 4 + dt * 2 + 1) * 1024 + lane16);
#pragma unroll
    for (int d = 0; d < 4; ++d) {
      o[2 * d] += bflo(h0[d]); o[2 * d + 1] += bfhi(h0[d]); o[8 + 2 * d] += bflo(h1[d]); o[8 + 2 * d + 1] += bfhi(h1[d]);
    }
    u16* op = out + (size_t)(cl * 64 + tt * 32 + r32) * DM + km * 64 + dt * 32 + hh * 8;
    *(v4u*)op = pack_lo(o);
    *(v4u*)(op + 16) = pack_hi(o);
  }
  if (b == 0 && tid == 0) __hip_atomic_store((gu64*)(ws + WS_EPOCH), tag, RLX_AGENT);
  STAMP(16);
}

extern "C" void kernel_launch(void* const* d_in, const int* in_sizes, int n_in, void* d_out, int out_size, void* d_ws, size_t ws_size,
                              hipStream_t stream) {
  (void)hipFuncSetAttribute((const void*)fnet_mega, hipFuncAttributeMaxDynamicSharedMemorySize, LDS_BYTES);
  fnet_mega<<<256, 256, LDS_BYTES, stream>>>((const u16*)d_in[0], (const u16*)d_in[1], (const u16*)d_in[2], (const u16*)d_in[3], (const u16*)d_in[4],
                                             (const u16*)d_in[6], (const u16*)d_in[7], (u16*)d_out, (char*)d_ws);
}
```

```cpp
#include <hip/hip_runtime.h>
#include <hip/hip_bf16.h>
#include <stdint.h>

#define DI __device__ __forceinline__
#define LAS __attribute__((address_space(3)))
typedef unsigned short u16;
typedef unsigned long long u64;
typedef unsigned v4u __attribute__((ext_vector_type(4)));
typedef unsigned v2u __attribute__((ext_vector_type(2)));
typedef float f32x16 __attribute__((ext_vector_type(16)));
typedef float f32x4 __attribute__((ext_vector_type(4)));
typedef float f32x2 __attribute__((ext_vector_type(2)));
typedef __bf16 bf16x2 __attribute__((ext_vector_type(2)));
typedef __amdgpu_buffer_rsrc_t rsrc_t;

#define CODE_PREFETCH 24000

constexpr int SEQ = 2048, DM = 512, FF = 1792;
constexpr unsigned WS_FLAG = 4096;
constexpr unsigned FL_AB = 0;
constexpr unsigned FL_EX = 1024;
constexpr unsigned FL_H = 5120;
constexpr unsigned FL_U = 6144;
constexpr unsigned FL_W = 13312;
constexpr unsigned FL_END = 14336;
constexpr unsigned WS_AB = 2u << 20;
constexpr unsigned WS_EX = 6u << 20;
constexpr unsigned WS_H = 14u << 20;
constexpr unsigned WS_HS = 16u << 20;
constexpr unsigned WS_U = 17u << 20;
constexpr unsigned WS_WT = 24u << 20;
constexpr unsigned WS_CS = WS_HS + (512u << 10);
constexpr unsigned WS_BYTES = 28u << 20;
constexpr unsigned OOB = 0x7ffffff0u;
static_assert(WS_FLAG + FL_END * 128 <= WS_AB, "flags");
constexpr int LDS_BYTES = 160 * 1024;
constexpr unsigned SPIN_CAP = 200000;

DI unsigned rfl(unsigned x) { return __builtin_amdgcn_readfirstlane(x); }
DI rsrc_t mk_rsrc(const void* p, unsigned bytes) { return __builtin_amdgcn_make_buffer_rsrc((void*)p, 0, bytes, 0x00020000); }
DI unsigned cvtpk(float lo, float hi) { f32x2 v = {lo, hi}; bf16x2 b = __builtin_convertvector(v, bf16x2); return __builtin_bit_cast(unsigned, b); }
DI float bflo(unsigned v) { return __uint_as_float(v << 16); }
DI float bfhi(unsigned v) { return __uint_as_float(v & 0xffff0000u); }
DI float rbf(float f) { return bflo(cvtpk(f, f)); }
DI float dot2(unsigned a, unsigned b, float c) {
  return __builtin_amdgcn_fdot2_f32_bf16(__builtin_bit_cast(bf16x2, a), __builtin_bit_cast(bf16x2, b), c, false);
}
#define WAIT_VM(n) asm volatile("s_waitcnt vmcnt(" #n ")" ::: "memory")
#define WAIT_VL(n, m) asm volatile("s_waitcnt vmcnt(" #n ") lgkmcnt(" #m ")" ::: "memory")
#define WAIT_LG(n) asm volatile("s_waitcnt lgkmcnt(" #n ")" ::: "memory")
#define BARRIER() asm volatile("s_barrier" ::: "memory")
#define PIN(x) asm volatile("" : "+v"(x))
#define GLDS(rs, voff, soff, lds, imm) asm volatile("s_add_u32 m0, %0, " #imm "\n\ts_nop 0\n\tbuffer_load_dwordx4 %1, %2, %3 offen lds" ::"s"(lds), "v"(voff), "s"(rs), "s"(soff) : "memory", "scc")
#define GLDS1(rs, voff, soff, lds, imm) asm volatile("s_add_u32 m0, %0, " #imm "\n\ts_nop 0\n\tbuffer_load_dwordx4 %1, %2, %3 offen sc1 lds" ::"s"(lds), "v"(voff), "s"(rs), "s"(soff) : "memory", "scc")
#define SET_M0(lds, imm) asm volatile("s_add_u32 m0, %0, " #imm ::"s"(lds) : "memory", "scc")
#define GLDSN(rs, voff, soff) asm volatile("buffer_load_dwordx4 %0, %1, %2 offen lds" ::"v"(voff), "s"(rs), "s"(soff) : "memory")
#define LD_A(r0, r1, imm, rs, voff, soff) asm volatile("buffer_load_dwordx4 a[" #r0 ":" #r1 "], %0, %1, %2 offen offset:" #imm " sc1" ::"v"(voff), "s"(rs), "s"(soff) : "memory")
#define LD_R(reg, imm, rs, voff, soff) asm volatile("buffer_load_dwordx4 " reg ", %0, %1, %2 offen offset:" #imm " sc1" ::"v"(voff), "s"(rs), "s"(soff) : "memory")
#define LD_RP(reg, rs, voff, soff) asm volatile("buffer_load_dwordx4 " reg ", %0, %1, %2 offen" ::"v"(voff), "s"(rs), "s"(soff) : "memory")
#define DS_WR_R(reg, addr, off) asm volatile("ds_write_b128 %0, " reg " offset:" #off ::"v"(addr) : "memory")
#define ST_R(reg, imm, rs, voff, soff) asm volatile("buffer_store_dwordx4 " reg ", %0, %1, %2 offen offset:" #imm " sc1" ::"v"(voff), "s"(rs), "s"(soff) : "memory")
#define MFMA_B(acc, a, reg) asm volatile("v_mfma_f32_32x32x16_bf16 %0, %1, " reg ", %0" : "+v"(acc) : "v"(a))
#define MFMA_BZ(acc, a, reg) asm volatile("v_mfma_f32_32x32x16_bf16 %0, %1, " reg ", 0" : "=&v"(acc) : "v"(a))
#define LD_V(d, imm, rs, voff, soff) asm volatile("buffer_load_dwordx4 %0, %1, %2, %3 offen offset:" #imm " sc1" : "=v"(d) : "v"(voff), "s"(rs), "s"(soff) : "memory")
#define LD_VP(d, rs, voff, soff) asm volatile("buffer_load_dwordx4 %0, %1, %2, %3 offen" : "=v"(d) : "v"(voff), "s"(rs), "s"(soff) : "memory")
#define LD_F(d, rs, voff) asm volatile("buffer_load_dwordx2 %0, %1, %2, 0 offen sc1" : "=v"(d) : "v"(voff), "s"(rs) : "memory")
#define DS_RD(d, addr, off) asm volatile("ds_read_b128 %0, %1 offset:" #off : "=v"(d) : "v"(addr) : "memory")
#define DS_RD2(d, addr, o0, o1) asm volatile("ds_read2st64_b32 %0, %1 offset0:" #o0 " offset1:" #o1 : "=v"(d) : "v"(addr) : "memory")
#define DS_TR_A(reg, addr, off) asm volatile("ds_read_b64_tr_b16 " reg ", %0 offset:" #off ::"v"(addr) : "memory")
#define MFMA_A(acc, reg, b) asm volatile("v_mfma_f32_32x32x16_bf16 %0, " reg ", %1, %0" : "+v"(acc) : "v"(b))
#define MFMA_AZ(acc, reg, b) asm volatile("v_mfma_f32_32x32x16_bf16 %0, " reg ", %1, 0" : "=&v"(acc) : "v"(b))
#define MFMA_V(acc, a, b) asm volatile("v_mfma_f32_32x32x16_bf16 %0, %1, %2, %0" : "+v"(acc) : "v"(a), "v"(b))
#define MFMA_VZ(acc, a, b) asm volatile("v_mfma_f32_32x32x16_bf16 %0, %1, %2, 0" : "=&v"(acc) : "v"(a), "v"(b))
#define MFMA_DONE(a) asm volatile("s_nop 15\n\ts_nop 3" : "+v"(a))
#define STAT2(s0, q0, s1, q1, x) do { float t_, u_; asm volatile("v_lshlrev_b32 %0, 16, %6\n\tv_and_b32 %1, 0xffff0000, %6\n\tv_add_f32 %2, %2, %0\n\tv_fmac_f32 %3, %0, %0\n\tv_add_f32 %4, %4, %1\n\tv_fmac_f32 %5, %1, %1" : "=&v"(t_), "=&v"(u_), "+v"(s0), "+v"(q0), "+v"(s1), "+v"(q1) : "v"(x)); } while (0)
DI void st_sc1(rsrc_t rs, v4u v, unsigned voff, unsigned soff) {
  asm volatile("buffer_store_dwordx4 %0, %1, %2, %3 offen sc1\n\ts_nop 1" ::"v"(v), "v"(voff), "s"(rs), "s"(soff) : "memory");
}
DI void st_flag(rsrc_t rs, u64 tag, unsigned voff) { __builtin_amdgcn_raw_buffer_store_b64(__builtin_bit_cast(v2u, tag), rs, voff, 0, 16); }
DI void poll(rsrc_t rs, unsigned voff, u64 tag) {
#pragma unroll 1
  for (unsigned s = 0; s < SPIN_CAP; ++s) {
    u64 v;
    asm volatile("buffer_load_dwordx2 %0, %1, %2, 0 offen sc1\n\ts_waitcnt vmcnt(0)" : "=v"(v) : "v"(voff), "s"(rs) : "memory");
    if (__all(v == tag)) break;
  }
}
DI unsigned relu_pk(unsigned v) { unsigned r; asm("v_pk_max_i16 %0, %1, 0" : "=v"(r) : "v"(v)); return r; }
DI v4u pack_lo(const f32x16& a) { v4u r = {cvtpk(a[0], a[1]), cvtpk(a[2], a[3]), cvtpk(a[4], a[5]), cvtpk(a[6], a[7])}; return r; }
DI v4u pack_hi(const f32x16& a) { v4u r = {cvtpk(a[8], a[9]), cvtpk(a[10], a[11]), cvtpk(a[12], a[13]), cvtpk(a[14], a[15])}; return r; }
DI int swap23(int r) { return (r & ~12) | ((r & 4) << 1) | ((r & 8) >> 1); }
DI void ln_scale(float s, float q, float& rs, float& mn) {
  float m = s * (1.0f / DM);
  float var = fmaxf(q * (1.0f / DM) - m * m, 0.f);
  float sd = rbf(__builtin_amdgcn_sqrtf(rbf(rbf(var) + 1e-5f)));
  rs = __builtin_amdgcn_rcpf(sd);
  mn = rbf(m);
}
#define FOFF(i) (WS_FLAG + (i) * 128)

#define HSOFF lane * 4, WS_HS + hpart * 256
#define HSA lane * 4
#define ROT1 (xcd & 6) + 4 * (xcd & 1) + ((jt ^ (idx >> 2)) & 1)
#define Z1V (xcd & 6)
#define FXV (xcd & 1)
#define HROT km
#define ROT2I 8 * (xcd & 1)
#define GROT (km + (xcd & 6)) % 7
__global__ __launch_bounds__(256, 1) __attribute__((amdgpu_num_vgpr(160))) void fnet_mega(const u16* __restrict__ x, const u16* __restrict__ cr, const u16* __restrict__ ci,
                                                   const u16* __restrict__ wr, const u16* __restrict__ wi, const u16* __restrict__ w1,
                                                   const u16* __restrict__ w2, u16* __restrict__ out, char* ws) {
  extern __shared__ __attribute__((aligned(16))) char smem[];
  const int b = blockIdx.x, xcd = b & 7, idx = b >> 3;
  const int tid = threadIdx.x, lane = tid & 63, w = rfl(tid >> 6);
  const unsigned lds0 = (unsigned)(uintptr_t)(LAS char*)smem;
  asm volatile("" ::: "a255", "v255");
  {
    u64 pc;
    asm volatile("s_getpc_b64 %0" : "=s"(pc));
    unsigned co = ((idx * 4 + w) * 2 + (lane & 1)) * 128;
    asm volatile("global_load_dword a255, %0, %1" ::"v"(co < CODE_PREFETCH ? co : 0u), "s"(pc) : "memory");
  }
  const int r32 = lane & 31, hh = lane >> 5;
  const unsigned lane16 = lane * 16;
  const unsigned ONES = 0x3f803f80u;
  const int sp = (xcd >> 1) * 8 + (idx >> 2), ch = xcd & 1, jt = idx & 3;
  const int q = xcd >> 1, pi = (xcd & 1) * 8 + (idx >> 2), pj = idx & 3;
  const int crot = idx >> 3, cl = xcd * 4 + crot, km = idx & 7;
  unsigned vs[6];
#pragma unroll
  for (int p = 0; p < 6; ++p) { int rho = (p < 2 ? 2 * w + p : 4 * w + p - 2) * 8 + (lane >> 3); vs[p] = swap23(rho) * 1024 + (((lane & 7) ^ ((rho >> 1) & 7)) << 4); PIN(vs[p]); }
  const rsrc_t rx = mk_rsrc(x + (size_t)sp * 64 * DM, 64 * 1024);
  const rsrc_t rw = mk_rsrc((ch ? wi : wr) + (size_t)jt * 128 * DM, 128 * 1024);
  const int rot = (ROT1) & 7;
  const unsigned lw2 = lds0 + w * 2048, lw4 = lds0 + w * 4096, lw8 = lds0 + w * 8192;
  GLDS(rx, vs[0], ((0 + rot) & 7) * 128, lw2, 49152);
  GLDS(rx, vs[1], ((0 + rot) & 7) * 128, lw2, 50176);
  GLDS(rw, vs[2], ((0 + rot) & 7) * 128, lw4, 57344);
  GLDS(rw, vs[3], ((0 + rot) & 7) * 128, lw4, 58368);
  GLDS(rw, vs[4], ((0 + rot) & 7) * 128, lw4, 59392);
  GLDS(rw, vs[5], ((0 + rot) & 7) * 128, lw4, 60416);
  GLDS(rx, vs[0], ((1 + rot) & 7) * 128, lw2, 73728);
  GLDS(rx, vs[1], ((1 + rot) & 7) * 128, lw2, 74752);
  GLDS(rw, vs[2], ((1 + rot) & 7) * 128, lw4, 81920);
  GLDS(rw, vs[3], ((1 + rot) & 7) * 128, lw4, 82944);
  GLDS(rw, vs[4], ((1 + rot) & 7) * 128, lw4, 83968);
  GLDS(rw, vs[5], ((1 + rot) & 7) * 128, lw4, 84992);
  asm volatile("" ::"s"(cr), "s"(ci), "s"(w1), "s"(w2), "s"(out), "s"(ws));
  const rsrc_t rws = mk_rsrc(ws, WS_BYTES);
  u64 ep;
  LD_F(ep, rws, 0u);
  { const int un_ = b * 4 + w;
    const unsigned o_ = lane == 0 ? FOFF(FL_AB + (((ch * 2 + (sp >> 4)) * 4 + jt) * 4 + w) * 16 + (sp & 15))
      : lane < 4 ? FOFF(FL_EX + (((pi * 4 + pj) * 4 + (q ^ lane)) * 4 + w) * 4 + lane - 1)
      : lane < 8 ? FOFF(FL_U + cl * 128 + (lane - 4) * 32 + km * 4 + w)
      : lane == 8 ? FOFF(FL_H + (pi * 4 + q) * 16 + pj * 4 + w) : lane == 9 ? FOFF(FL_W + un_)
      : OOB;
    asm volatile("buffer_load_dword a249, %0, %1, 0 offen" ::"v"(o_), "s"(rws) : "memory"); }
  const rsrc_t rc = mk_rsrc((q >> 1 ? ci : cr) + (size_t)pi * 128 * SEQ + (q & 1) * 1024, 128 * 4096 - (q & 1) * 2048);
  const int rot2 = (4 * pj + ROT2I) & 15;
  unsigned vc[4];
#pragma unroll
  for (int T = 0; T < 4; ++T) { int rho = w * 8 + (lane >> 3); vc[T] = ((T ^ q) * 32 + rho) * 4096 + (((lane & 7) ^ ((rho >> 1) & 7)) << 4); }
  const unsigned lw1 = lds0 + w * 1024;
  unsigned fb0[4];
#pragma unroll
  for (int k = 0; k < 4; ++k) fb0[k] = lds0 + r32 * 128 + (((2 * k + hh) ^ ((r32 >> 1) & 7)) << 4);
  unsigned ra[2][4], rb[2][4], rsa[2];
#pragma unroll
  for (int k = 0; k < 4; ++k) { ra[0][k] = fb0[k] + 49152; ra[1][k] = fb0[k] + 98304; rb[0][k] = ra[0][k] + w * 4096; rb[1][k] = ra[1][k] + w * 4096; }
  rsa[0] = lds0 + 49152 + (tid >> 2) * 128 + (tid & 3) * 16; rsa[1] = rsa[0] + 49152;
  f32x16 acc0, acc1, acc2, acc3;
  v4u ones = {ONES, ONES, ONES, ONES};
  PIN(ones);
  float s0 = 0.f, s1 = 0.f, q0 = 0.f, q1 = 0.f;
  v4u fb[3], fa0[3], fa1[3], fs[4];
  const int unit = b * 4 + w;
  const bool is_w2 = unit >= 448;
  const rsrc_t rst = mk_rsrc(ws, unit < 896 ? WS_BYTES : 0);
  const unsigned wld = is_w2 ? DM * 2 : FF * 2;
  const unsigned wso = is_w2 ? ((unit - 448) % 56) * 32 * (DM * 2) + ((unit - 448) / 56) * 128 : (unit & 15) * 32 * (FF * 2) + (unit >> 4) * 128;
  const rsrc_t rwt = mk_rsrc(is_w2 ? w2 : w1, unit < 896 ? DM * FF * 2 : 0);
  const unsigned vwt = (lane >> 3) * wld + (((lane & 7) ^ (lane >> 2 & 4)) << 4);
  WAIT_VM(8);
  BARRIER();
  DS_RD(fb[0], rb[0][0], 8192);
  DS_RD(fa0[0], ra[0][0], 0);
  DS_RD(fa1[0], ra[0][0], 4096);
  DS_RD(fb[1], rb[0][1], 8192);
  DS_RD(fa0[1], ra[0][1], 0);
  DS_RD(fa1[1], ra[0][1], 4096);
  DS_RD(fs[0], rsa[0], 0);
  DS_RD(fs[1], rsa[0], 64);
  float* stat = (float*)(smem + 44 * 1024);
  const unsigned sta = lds0 + 44 * 1024 + hh * 16; v4u sr[8], sm[8];
  WAIT_LG(5);
  MFMA_VZ(acc2, ones, fb[0]);
  SET_M0(lw2, 98304);
  DS_RD(fb[2], rb[0][2], 8192);
  GLDSN(rx, vs[0], ((2 + rot) & 7) * 128);
  MFMA_VZ(acc0, fa0[0], fb[0]);
  SET_M0(lw2, 99328);
  DS_RD(fa0[2], ra[0][2], 0);
  GLDSN(rx, vs[1], ((2 + rot) & 7) * 128);
  MFMA_VZ(acc1, fa1[0], fb[0]);
  SET_M0(lw4, 106496);
  DS_RD(fa1[2], ra[0][2], 4096);
  GLDSN(rw, vs[2], ((2 + rot) & 7) * 128);
  WAIT_LG(5);
  WAIT_LG(3);
  MFMA_V(acc2, ones, fb[1]);
  SET_M0(lw4, 107520);
  DS_RD(fb[0], rb[0][3], 8192);
  GLDSN(rw, vs[3], ((2 + rot) & 7) * 128);
  STAT2(s0, q0, s1, q1, fs[0][0]);
  MFMA_V(acc0, fa0[1], fb[1]);
  SET_M0(lw4, 108544);
  DS_RD(fa0[0], ra[0][3], 0);
  GLDSN(rw, vs[4], ((2 + rot) & 7) * 128);
  STAT2(s0, q0, s1, q1, fs[0][1]);
  MFMA_V(acc1, fa1[1], fb[1]);
  SET_M0(lw4, 109568);
  DS_RD(fa1[0], ra[0][3], 4096);
  GLDSN(rw, vs[5], ((2 + rot) & 7) * 128);
  STAT2(s0, q0, s1, q1, fs[0][2]);
  WAIT_VM(8);
  BARRIER();
  WAIT_LG(3);
  MFMA_V(acc2, ones, fb[2]);
  DS_RD(fs[2], rsa[0], 24576);
  DS_RD(fs[3], rsa[0], 24640);
  SET_M0(lw2, 122880);
  DS_RD(fb[1], rb[0][0], 32768);
  GLDSN(rx, vs[0], ((3 + rot) & 7) * 128);
  STAT2(s0, q0, s1, q1, fs[0][3]);
  MFMA_V(acc0, fa0[2], fb[2]);
  DS_RD(fa0[1], ra[0][0], 24576);
  STAT2(s0, q0, s1, q1, fs[1][0]);
  MFMA_V(acc1, fa1[2], fb[2]);
  SET_M0(lw2, 123904);
  DS_RD(fa1[1], ra[0][0], 28672);
  GLDSN(rx, vs[1], ((3 + rot) & 7) * 128);
  STAT2(s0, q0, s1, q1, fs[1][1]);
  WAIT_LG(5);
  WAIT_LG(3);
  MFMA_V(acc2, ones, fb[0]);
  DS_RD(fb[2], rb[0][1], 32768);
  STAT2(s0, q0, s1, q1, fs[1][2]);
  MFMA_V(acc0, fa0[0], fb[0]);
  SET_M0(lw4, 131072);
  DS_RD(fa0[2], ra[0][1], 24576);
  GLDSN(rw, vs[2], ((3 + rot) & 7) * 128);
  STAT2(s0, q0, s1, q1, fs[1][3]);
  MFMA_V(acc1, fa1[0], fb[0]);
  DS_RD(fa1[2], ra[0][1], 28672);
  STAT2(s0, q0, s1, q1, fs[2][0]);
  WAIT_LG(3);
  MFMA_V(acc2, ones, fb[1]);
  SET_M0(lw4, 132096);
  DS_RD(fb[0], rb[0][2], 32768);
  GLDSN(rw, vs[3], ((3 + rot) & 7) * 128);
  STAT2(s0, q0, s1, q1, fs[2][1]);
  MFMA_V(acc0, fa0[1], fb[1]);
  DS_RD(fa0[0], ra[0][2], 24576);
  STAT2(s0, q0, s1, q1, fs[2][2]);
  MFMA_V(acc1, fa1[1], fb[1]);
  SET_M0(lw4, 133120);
  DS_RD(fa1[0], ra[0][2], 28672);
  GLDSN(rw, vs[4], ((3 + rot) & 7) * 128);
  STAT2(s0, q0, s1, q1, fs[2][3]);
  WAIT_LG(3);
  MFMA_V(acc2, ones, fb[2]);
  DS_RD(fb[1], rb[0][3], 32768);
  STAT2(s0, q0, s1, q1, fs[3][0]);
  MFMA_V(acc0, fa0[2], fb[2]);
  SET_M0(lw4, 134144);
  DS_RD(fa0[1], ra[0][3], 24576);
  GLDSN(rw, vs[5], ((3 + rot) & 7) * 128);
  STAT2(s0, q0, s1, q1, fs[3][1]);
  MFMA_V(acc1, fa1[2], fb[2]);
  DS_RD(fa1[1], ra[0][3], 28672);
  STAT2(s0, q0, s1, q1, fs[3][2]);
  WAIT_VM(6);
  BARRIER();
  WAIT_LG(3);
  MFMA_V(acc2, ones, fb[0]);
  DS_RD(fs[0], rsa[1], 0);
  DS_RD(fs[1], rsa[1], 64);
  SET_M0(lw2, 49152);
  DS_RD(fb[2], rb[1][0], 8192);
  GLDSN(rx, vs[0], ((4 + rot) & 7) * 128);
  MFMA_V(acc0, fa0[0], fb[0]);
  DS_RD(fa0[2], ra[1][0], 0);
  STAT2(s0, q0, s1, q1, fs[3][3]);
  MFMA_V(acc1, fa1[0], fb[0]);
  SET_M0(lw2, 50176);
  DS_RD(fa1[2], ra[1][0], 4096);
  GLDSN(rx, vs[1], ((4 + rot) & 7) * 128);
  WAIT_LG(5);
  WAIT_LG(3);
  MFMA_V(acc2, ones, fb[1]);
  DS_RD(fb[0], rb[1][1], 8192);
  STAT2(s0, q0, s1, q1, fs[0][0]);
  MFMA_V(acc0, fa0[1], fb[1]);
  SET_M0(lw4, 57344);
  DS_RD(fa0[0], ra[1][1], 0);
  GLDSN(rw, vs[2], ((4 + rot) & 7) * 128);
  STAT2(s0, q0, s1, q1, fs[0][1]);
  MFMA_V(acc1, fa1[1], fb[1]);
  DS_RD(fa1[0], ra[1][1], 4096);
  STAT2(s0, q0, s1, q1, fs[0][2]);
  WAIT_LG(3);
  MFMA_V(acc2, ones, fb[2]);
  SET_M0(lw4, 58368);
  DS_RD(fb[1], rb[1][2], 8192);
  GLDSN(rw, vs[3], ((4 + rot) & 7) * 128);
  MFMA_V(acc0, fa0[2], fb[2]);
  DS_RD(fa0[1], ra[1][2], 0);
  STAT2(s0, q0, s1, q1, fs[0][3]);
  MFMA_V(acc1, fa1[2], fb[2]);
  SET_M0(lw4, 59392);
  DS_RD(fa1[1], ra[1][2], 4096);
  GLDSN(rw, vs[4], ((4 + rot) & 7) * 128);
  WAIT_LG(3);
  MFMA_V(acc2, ones, fb[0]);
  DS_RD(fb[2], rb[1][3], 8192);
  STAT2(s0, q0, s1, q1, fs[1][0]);
  MFMA_V(acc0, fa0[0], fb[0]);
  SET_M0(lw4, 60416);
  DS_RD(fa0[2], ra[1][3], 0);
  GLDSN(rw, vs[5], ((4 + rot) & 7) * 128);
  STAT2(s0, q0, s1, q1, fs[1][1]);
  MFMA_V(acc1, fa1[0], fb[0]);
  DS_RD(fa1[2], ra[1][3], 4096);
  STAT2(s0, q0, s1, q1, fs[1][2]);
  WAIT_VM(6);
  BARRIER();
  WAIT_LG(3);
  MFMA_V(acc2, ones, fb[1]);
  DS_RD(fs[2], rsa[1], 24576);
  DS_RD(fs[3], rsa[1], 24640);
  SET_M0(lw2, 73728);
  DS_RD(fb[0], rb[1][0], 32768);
  GLDSN(rx, vs[0], ((5 + rot) & 7) * 128);
  MFMA_V(acc0, fa0[1], fb[1]);
  DS_RD(fa0[0], ra[1][0], 24576);
  STAT2(s0, q0, s1, q1, fs[1][3]);
  MFMA_V(acc1, fa1[1], fb[1]);
  SET_M0(lw2, 74752);
  DS_RD(fa1[0], ra[1][0], 28672);
  GLDSN(rx, vs[1], ((5 + rot) & 7) * 128);
  WAIT_LG(5);
  WAIT_LG(3);
  MFMA_V(acc2, ones, fb[2]);
  DS_RD(fb[1], rb[1][1], 32768);
  STAT2(s0, q0, s1, q1, fs[2][0]);
  MFMA_V(acc0, fa0[2], fb[2]);
  SET_M0(lw4, 81920);
  DS_RD(fa0[1], ra[1][1], 24576);
  GLDSN(rw, vs[2], ((5 + rot) & 7) * 128);
  STAT2(s0, q0, s1, q1, fs[2][1]);
  MFMA_V(acc1, fa1[2], fb[2]);
  DS_RD(fa1[1], ra[1][1], 28672);
  STAT2(s0, q0, s1, q1, fs[2][2]);
  WAIT_LG(3);
  MFMA_V(acc2, ones, fb[0]);
  SET_M0(lw4, 82944);
  DS_RD(fb[2], rb[1][2], 32768);
  GLDSN(rw, vs[3], ((5 + rot) & 7) * 128);
  MFMA_V(acc0, fa0[0], fb[0]);
  DS_RD(fa0[2], ra[1][2], 24576);
  STAT2(s0, q0, s1, q1, fs[2][3]);
  MFMA_V(acc1, fa1[0], fb[0]);
  SET_M0(lw4, 83968);
  DS_RD(fa1[2], ra[1][2], 28672);
  GLDSN(rw, vs[4], ((5 + rot) & 7) * 128);
  WAIT_LG(3);
  MFMA_V(acc2, ones, fb[1]);
  DS_RD(fb[0], rb[1][3], 32768);
  STAT2(s0, q0, s1, q1, fs[3][0]);
  MFMA_V(acc0, fa0[1], fb[1]);
  SET_M0(lw4, 84992);
  DS_RD(fa0[0], ra[1][3], 24576);
  GLDSN(rw, vs[5], ((5 + rot) & 7) * 128);
  STAT2(s0, q0, s1, q1, fs[3][1]);
  MFMA_V(acc1, fa1[1], fb[1]);
  DS_RD(fa1[0], ra[1][3], 28672);
  STAT2(s0, q0, s1, q1, fs[3][2]);
  WAIT_VM(6);
  BARRIER();
  WAIT_LG(3);
  MFMA_V(acc2, ones, fb[2]);
  DS_RD(fs[0], rsa[0], 0);
  DS_RD(fs[1], rsa[0], 64);
  SET_M0(lw2, 98304);
  DS_RD(fb[1], rb[0][0], 8192);
  GLDSN(rx, vs[0], ((6 + rot) & 7) * 128);
  MFMA_V(acc0, fa0[2], fb[2]);
  DS_RD(fa0[1], ra[0][0], 0);
  STAT2(s0, q0, s1, q1, fs[3][3]);
  MFMA_V(acc1, fa1[2], fb[2]);
  SET_M0(lw2, 99328);
  DS_RD(fa1[1], ra[0][0], 4096);
  GLDSN(rx, vs[1], ((6 + rot) & 7) * 128);
  WAIT_LG(5);
  WAIT_LG(3);
  MFMA_V(acc2, ones, fb[0]);
  DS_RD(fb[2], rb[0][1], 8192);
  STAT2(s0, q0, s1, q1, fs[0][0]);
  MFMA_V(acc0, fa0[0], fb[0]);
  SET_M0(lw4, 106496);
  DS_RD(fa0[2], ra[0][1], 0);
  GLDSN(rw, vs[2], ((6 + rot) & 7) * 128);
  STAT2(s0, q0, s1, q1, fs[0][1]);
  MFMA_V(acc1, fa1[0], fb[0]);
  DS_RD(fa1[2], ra[0][1], 4096);
  STAT2(s0, q0, s1, q1, fs[0][2]);
  WAIT_LG(3);
  MFMA_V(acc2, ones, fb[1]);
  SET_M0(lw4, 107520);
  DS_RD(fb[0], rb[0][2], 8192);
  GLDSN(rw, vs[3], ((6 + rot) & 7) * 128);
  MFMA_V(acc0, fa0[1], fb[1]);
  DS_RD(fa0[0], ra[0][2], 0);
  STAT2(s0, q0, s1, q1, fs[0][3]);
  MFMA_V(acc1, fa1[1], fb[1]);
  SET_M0(lw4, 108544);
  DS_RD(fa1[0], ra[0][2], 4096);
  GLDSN(rw, vs[4], ((6 + rot) & 7) * 128);
  WAIT_LG(3);
  MFMA_V(acc2, ones, fb[2]);
  DS_RD(fb[1], rb[0][3], 8192);
  STAT2(s0, q0, s1, q1, fs[1][0]);
  MFMA_V(acc0, fa0[2], fb[2]);
  SET_M0(lw4, 109568);
  DS_RD(fa0[1], ra[0][3], 0);
  GLDSN(rw, vs[5], ((6 + rot) & 7) * 128);
  STAT2(s0, q0, s1, q1, fs[1][1]);
  MFMA_V(acc1, fa1[2], fb[2]);
  DS_RD(fa1[1], ra[0][3], 4096);
  STAT2(s0, q0, s1, q1, fs[1][2]);
  WAIT_VM(6);
  BARRIER();
  WAIT_LG(3);
  MFMA_V(acc2, ones, fb[0]);
  DS_RD(fs[2], rsa[0], 24576);
  DS_RD(fs[3], rsa[0], 24640);
  SET_M0(lw2, 122880);
  DS_RD(fb[2], rb[0][0], 32768);
  GLDSN(rx, vs[0], ((7 + rot) & 7) * 128);
  MFMA_V(acc0, fa0[0], fb[0]);
  DS_RD(fa0[2], ra[0][0], 24576);
  STAT2(s0, q0, s1, q1, fs[1][3]);
  MFMA_V(acc1, fa1[0], fb[0]);
  SET_M0(lw2, 123904);
  DS_RD(fa1[2], ra[0][0], 28672);
  GLDSN(rx, vs[1], ((7 + rot) & 7) * 128);
  WAIT_LG(5);
  WAIT_LG(3);
  MFMA_V(acc2, ones, fb[1]);
  DS_RD(fb[0], rb[0][1], 32768);
  STAT2(s0, q0, s1, q1, fs[2][0]);
  MFMA_V(acc0, fa0[1], fb[1]);
  SET_M0(lw4, 131072);
  DS_RD(fa0[0], ra[0][1], 24576);
  GLDSN(rw, vs[2], ((7 + rot) & 7) * 128);
  STAT2(s0, q0, s1, q1, fs[2][1]);
  MFMA_V(acc1, fa1[1], fb[1]);
  DS_RD(fa1[0], ra[0][1], 28672);
  STAT2(s0, q0, s1, q1, fs[2][2]);
  WAIT_LG(3);
  MFMA_V(acc2, ones, fb[2]);
  SET_M0(lw4, 132096);
  DS_RD(fb[1], rb[0][2], 32768);
  GLDSN(rw, vs[3], ((7 + rot) & 7) * 128);
  MFMA_V(acc0, fa0[2], fb[2]);
  DS_RD(fa0[1], ra[0][2], 24576);
  STAT2(s0, q0, s1, q1, fs[2][3]);
  MFMA_V(acc1, fa1[2], fb[2]);
  SET_M0(lw4, 133120);
  DS_RD(fa1[1], ra[0][2], 28672);
  GLDSN(rw, vs[4], ((7 + rot) & 7) * 128);
  WAIT_LG(3);
  MFMA_V(acc2, ones, fb[0]);
  DS_RD(fb[2], rb[0][3], 32768);
  STAT2(s0, q0, s1, q1, fs[3][0]);
  MFMA_V(acc0, fa0[0], fb[0]);
  SET_M0(lw4, 134144);
  DS_RD(fa0[2], ra[0][3], 24576);
  GLDSN(rw, vs[5], ((7 + rot) & 7) * 128);
  STAT2(s0, q0, s1, q1, fs[3][1]);
  MFMA_V(acc1, fa1[0], fb[0]);
  DS_RD(fa1[2], ra[0][3], 28672);
  STAT2(s0, q0, s1, q1, fs[3][2]);
  WAIT_VM(6);
  BARRIER();
  WAIT_LG(3);
  MFMA_V(acc2, ones, fb[1]);
  DS_RD(fs[0], rsa[1], 0);
  DS_RD(fs[1], rsa[1], 64);
  SET_M0(lw1, 0);
  DS_RD(fb[0], rb[1][0], 8192);
  GLDSN(rc, vc[1], ((0 + rot2) & 15) * 128);
  MFMA_V(acc0, fa0[1], fb[1]);
  DS_RD(fa0[0], ra[1][0], 0);
  STAT2(s0, q0, s1, q1, fs[3][3]);
  MFMA_V(acc1, fa1[1], fb[1]);
  SET_M0(lw1, 4096);
  DS_RD(fa1[0], ra[1][0], 4096);
  GLDSN(rc, vc[2], ((0 + rot2) & 15) * 128);
  WAIT_LG(5);
  WAIT_LG(3);
  MFMA_V(acc2, ones, fb[2]);
  DS_RD(fb[1], rb[1][1], 8192);
  STAT2(s0, q0, s1, q1, fs[0][0]);
  MFMA_V(acc0, fa0[2], fb[2]);
  SET_M0(lw1, 8192);
  DS_RD(fa0[1], ra[1][1], 0);
  GLDSN(rc, vc[3], ((0 + rot2) & 15) * 128);
  STAT2(s0, q0, s1, q1, fs[0][1]);
  MFMA_V(acc1, fa1[2], fb[2]);
  DS_RD(fa1[1], ra[1][1], 4096);
  STAT2(s0, q0, s1, q1, fs[0][2]);
  WAIT_LG(3);
  MFMA_V(acc2, ones, fb[0]);
  SET_M0(lw1, 12288);
  DS_RD(fb[2], rb[1][2], 8192);
  GLDSN(rc, vc[1], ((1 + rot2) & 15) * 128);
  MFMA_V(acc0, fa0[0], fb[0]);
  DS_RD(fa0[2], ra[1][2], 0);
  STAT2(s0, q0, s1, q1, fs[0][3]);
  MFMA_V(acc1, fa1[0], fb[0]);
  SET_M0(lw1, 16384);
  DS_RD(fa1[2], ra[1][2], 4096);
  GLDSN(rc, vc[2], ((1 + rot2) & 15) * 128);
  WAIT_LG(3);
  MFMA_V(acc2, ones, fb[1]);
  DS_RD(fb[0], rb[1][3], 8192);
  STAT2(s0, q0, s1, q1, fs[1][0]);
  MFMA_V(acc0, fa0[1], fb[1]);
  SET_M0(lw1, 20480);
  DS_RD(fa0[0], ra[1][3], 0);
  GLDSN(rc, vc[3], ((1 + rot2) & 15) * 128);
  STAT2(s0, q0, s1, q1, fs[1][1]);
  MFMA_V(acc1, fa1[1], fb[1]);
  DS_RD(fa1[0], ra[1][3], 4096);
  STAT2(s0, q0, s1, q1, fs[1][2]);
  WAIT_VM(6);
  BARRIER();
  WAIT_LG(3);
  MFMA_V(acc2, ones, fb[2]);
  DS_RD(fs[2], rsa[1], 24576);
  DS_RD(fs[3], rsa[1], 24640);
  SET_M0(lw1, 24576);
  DS_RD(fb[1], rb[1][0], 32768);
  GLDSN(rc, vc[1], ((2 + rot2) & 15) * 128);
  MFMA_V(acc0, fa0[2], fb[2]);
  DS_RD(fa0[1], ra[1][0], 24576);
  STAT2(s0, q0, s1, q1, fs[1][3]);
  MFMA_V(acc1, fa1[2], fb[2]);
  SET_M0(lw1, 28672);
  DS_RD(fa1[1], ra[1][0], 28672);
  GLDSN(rc, vc[2], ((2 + rot2) & 15) * 128);
  WAIT_LG(5);
  WAIT_LG(3);
  MFMA_V(acc2, ones, fb[0]);
  DS_RD(fb[2], rb[1][1], 32768);
  STAT2(s0, q0, s1, q1, fs[2][0]);
  STAT2(s0, q0, s1, q1, fs[2][1]);
  MFMA_V(acc0, fa0[0], fb[0]);
  SET_M0(lw1, 32768);
  DS_RD(fa0[2], ra[1][1], 24576);
  GLDSN(rc, vc[3], ((2 + rot2) & 15) * 128);
  STAT2(s0, q0, s1, q1, fs[2][2]);
  MFMA_V(acc1, fa1[0], fb[0]);
  DS_RD(fa1[2], ra[1][1], 28672);
  STAT2(s0, q0, s1, q1, fs[2][3]);
  WAIT_LG(3);
  MFMA_V(acc2, ones, fb[1]);
  SET_M0(lw1, 36864);
  DS_RD(fb[0], rb[1][2], 32768);
  GLDSN(rc, vc[1], ((3 + rot2) & 15) * 128);
  STAT2(s0, q0, s1, q1, fs[3][0]);
  STAT2(s0, q0, s1, q1, fs[3][1]);
  MFMA_V(acc0, fa0[1], fb[1]);
  DS_RD(fa0[0], ra[1][2], 24576);
  STAT2(s0, q0, s1, q1, fs[3][2]);
  MFMA_V(acc1, fa1[1], fb[1]);
  SET_M0(lw1, 40960);
  DS_RD(fa1[0], ra[1][2], 28672);
  GLDSN(rc, vc[2], ((3 + rot2) & 15) * 128);
  STAT2(s0, q0, s1, q1, fs[3][3]);
  WAIT_LG(3);
  MFMA_V(acc2, ones, fb[2]);
  DS_RD(fb[1], rb[1][3], 32768);
  s0 += s1; q0 += q1;
  s0 += __builtin_amdgcn_update_dpp(0.f, s0, 0xB1, 0xf, 0xf, true); q0 += __builtin_amdgcn_update_dpp(0.f, q0, 0xB1, 0xf, 0xf, true);
  s0 += __builtin_amdgcn_update_dpp(0.f, s0, 0x4E, 0xf, 0xf, true); q0 += __builtin_amdgcn_update_dpp(0.f, q0, 0x4E, 0xf, 0xf, true);
  PIN(s0); PIN(q0);
  MFMA_V(acc0, fa0[2], fb[2]);
  DS_RD(fa0[1], ra[1][3], 24576);
  { float rs, mn; ln_scale(s0, q0, rs, mn); if (ch) rs = -rs; stat[(tid & 1) * 64 + (tid >> 2)] = (tid & 1) ? mn : rs; }
  MFMA_V(acc1, fa1[2], fb[2]);
  DS_RD(fa1[1], ra[1][3], 28672);
  WAIT_LG(3);
  WAIT_LG(0);
  WAIT_LG(0);
  BARRIER();
  MFMA_V(acc2, ones, fb[0]);
  DS_RD(sr[0], sta, 0);
  DS_RD(sm[0], sta, 256);
  DS_RD(sr[1], sta, 32);
  MFMA_V(acc0, fa0[0], fb[0]);
  DS_RD(sm[1], sta, 288);
  DS_RD(sr[2], sta, 64);
  DS_RD(sm[2], sta, 320);
  MFMA_V(acc1, fa1[0], fb[0]);
  DS_RD(sr[3], sta, 96);
  DS_RD(sm[3], sta, 352);
  DS_RD(sr[4], sta, 128);
  MFMA_V(acc2, ones, fb[1]);
  DS_RD(sm[4], sta, 384);
  DS_RD(sr[5], sta, 160);
  DS_RD(sm[5], sta, 416);
  MFMA_V(acc0, fa0[1], fb[1]);
  DS_RD(sr[6], sta, 192);
  DS_RD(sm[6], sta, 448);
  DS_RD(sr[7], sta, 224);
  MFMA_V(acc1, fa1[1], fb[1]);
  DS_RD(sm[7], sta, 480);
  WAIT_LG(0);
  PIN(sr[0]);
  PIN(sr[1]);
  PIN(sr[2]);
  PIN(sr[3]);
  PIN(sr[4]);
  PIN(sr[5]);
  PIN(sr[6]);
  PIN(sr[7]);
  PIN(sm[0]);
  PIN(sm[1]);
  PIN(sm[2]);
  PIN(sm[3]);
  PIN(sm[4]);
  PIN(sm[5]);
  PIN(sm[6]);
  PIN(sm[7]);
  MFMA_DONE(acc1); PIN(acc0); PIN(acc2);
  const unsigned abi = ((ch * 2 + (sp >> 4)) * 4 + jt);
  auto fin = [&](f32x16& acc, int mt) {
    const float nws = -acc2[0];
#pragma unroll
    for (int g = 0; g < 4; ++g) {
      f32x4 r4 = __builtin_bit_cast(f32x4, sr[mt * 4 + g]), m4 = __builtin_bit_cast(f32x4, sm[mt * 4 + g]);
#pragma unroll
      for (int e = 0; e < 4; ++e) acc[4 * g + e] = __builtin_fmaf(m4[e], nws, acc[4 * g + e]) * r4[e];
    }
    st_sc1(rws, pack_lo(acc), lane16 + mt * 2048, WS_AB + ((abi * 16 + (sp & 15)) * 4 + w) * 4096);
    st_sc1(rws, pack_hi(acc), lane16 + mt * 2048 + 1024, WS_AB + ((abi * 16 + (sp & 15)) * 4 + w) * 4096);
  };
  fin(acc0, 0); fin(acc1, 1);
  BARRIER();
  PIN(ep);
  const u64 tag = ep * 6364136223846793005ull + 1442695040888963407ull;
  v4u xr0, xr1;
  const rsrc_t rxa = mk_rsrc(x, SEQ * DM * 2);
  const int kmc = km < 7 ? km : 6;
  const rsrc_t rff = mk_rsrc(ws, km < 7 ? WS_BYTES : 0);
  const int fx = FXV;
  const int z1 = Z1V;
  const unsigned w1o = WS_WT + (kmc * 4 + w) * 65536, w2o = WS_WT + (448 + km * 56 + w * 14) * 4096;
  const unsigned va0 = ((lane ^ ((w & 1) << 4)) * 16) + (w >> 1) * 1024, va1 = va0 ^ 1024;
  const int grot = GROT;
  auto grp = [&](int g) { int v = g + grot; return v >= 7 ? v - 7 : v; };
  const unsigned fw_in = FOFF(FL_W + (lane < 16 ? (kmc * 4 + w) * 16 + lane : 448 + km * 56 + w * 14 + (lane < 30 ? lane - 16 : 0)));
  const unsigned wto = is_w2 ? WS_WT + unit * 4096 : WS_WT + (unit >> 4) * 65536 + (unit & 15) * 2048;
  const unsigned wtk = is_w2 ? 2048 : 1024, wtn = is_w2 ? 1024 : 32768;
  unsigned tb0, tb1;
  { const int G = lane >> 4, qq = (lane >> 2) & 3, pp = lane & 3;
    tb0 = lds0 + 144 * 1024 + w * 4096 + (8 * (G >> 1) + qq) * 128 + (8 * (pp & 1) + 4 * (pp >> 1)) * 2 + (qq >> 1) * 64 + 32 * (G & 1); tb1 = tb0 ^ 64; }
  const unsigned exb = WS_EX + (pi * 4 + pj) * 131072;
  const unsigned fex_out = FOFF(FL_EX + (((pi * 4 + pj) * 4 + (q ^ ((lane >> 4) + 1))) * 4 + w) * 4 + (lane >> 4)) + (lane & 15) * 8;
  const unsigned exi = exb + (q * 16 + w) * 2048;
  const unsigned fex_in = FOFF(FL_EX + (((pi * 4 + pj) * 4 + q) * 4 + w) * 4 + (lane < 3 ? lane : 2));
  const int hpart = (pi * 4 + q) * 16 + pj * 4 + w;
  const int hblk = w ^ (z1 >> 1);
  const unsigned fh_in = FOFF(FL_H + (cl * 2 + ((lane >> 2) & 1)) * 16 + hblk * 4 + (lane & 3));
  const unsigned fu_in = FOFF(FL_U + cl * 128 + w * 7 + (lane < 7 ? lane : 6)), fu_out = lane < 16 ? FOFF(FL_U + cl * 128 + km * 4 + w) + lane * 8 : OOB;
  const unsigned abo = WS_AB + (q * 4 + pj) * 16 * 16384 + w * 4096;
  unsigned cb1[4], cb2[4];
#pragma unroll
  for (int k = 0; k < 4; ++k) { cb1[k] = fb0[k] + 65536; cb2[k] = fb0[k] + 131072; }
  v4u bf[12], ex[6], hres0, hres1, cp[4];
  f32x16 cs0, cs1; v4u pk0, pk1; f32x4 cy;
  u64 fv, fw;
  const unsigned fab_in = FOFF(FL_AB + ((q * 4 + pj) * 4 + w) * 16 + (lane & 15));

  GLDS(rc, vc[3], ((3 + rot2) & 15) * 128, lw1, 45056);
  asm volatile("buffer_load_dword a254, %0, %1, %2 offen" ::"v"((q * 32 + w * 8 + (lane >> 3)) * 4096 + (lane & 3) * 128), "s"(rc), "s"(rot2 * 128) : "memory");
  GLDS(rwt, vwt, wso + 0 * wld, lw4, 147456);
  GLDS(rwt, vwt, wso + 8 * wld, lw4, 148480);
  GLDS(rwt, vwt, wso + 16 * wld, lw4, 149504);
  GLDS(rwt, vwt, wso + 24 * wld, lw4, 150528);
  LD_VP(xr0, rxa, r32 * 1024 + hh * 16, (pi * 128 + q * 32) * 1024 + (pj * 128 + w * 32) * 2);
  LD_VP(xr1, rxa, r32 * 1024 + hh * 16, (pi * 128 + q * 32) * 1024 + (pj * 128 + w * 32 + 16) * 2);
  WAIT_VM(8);
  st_flag(rws, tag, lane < 16 ? FOFF(FL_AB + (abi * 4 + w) * 16 + (sp & 15)) + lane * 8 : OOB);
  GLDS(rc, vc[1], ((4 + rot2) & 15) * 128, lw1, 49152);
  GLDS(rc, vc[2], ((4 + rot2) & 15) * 128, lw1, 53248);
  GLDS(rc, vc[3], ((4 + rot2) & 15) * 128, lw1, 57344);
  GLDS(rc, vc[1], ((5 + rot2) & 15) * 128, lw1, 61440);
  GLDS(rc, vc[2], ((5 + rot2) & 15) * 128, lw1, 65536);
  GLDS(rc, vc[3], ((5 + rot2) & 15) * 128, lw1, 69632);
  GLDS(rc, vc[1], ((6 + rot2) & 15) * 128, lw1, 73728);
  GLDS(rc, vc[2], ((6 + rot2) & 15) * 128, lw1, 77824);
  GLDS(rc, vc[3], ((6 + rot2) & 15) * 128, lw1, 81920);
  GLDS(rc, vc[1], ((7 + rot2) & 15) * 128, lw1, 86016);
  GLDS(rc, vc[2], ((7 + rot2) & 15) * 128, lw1, 90112);
  GLDS(rc, vc[3], ((7 + rot2) & 15) * 128, lw1, 94208);
  GLDS(rc, vc[1], ((8 + rot2) & 15) * 128, lw1, 98304);
  GLDS(rc, vc[2], ((8 + rot2) & 15) * 128, lw1, 102400);
  GLDS(rc, vc[3], ((8 + rot2) & 15) * 128, lw1, 106496);
  GLDS(rc, vc[1], ((9 + rot2) & 15) * 128, lw1, 110592);
  WAIT_VM(19);
  DS_TR_A("v[240:241]", tb0, 0);
  DS_TR_A("v[242:243]", tb0, 512);
  DS_TR_A("v[244:245]", tb1, 0);
  DS_TR_A("v[246:247]", tb1, 512);
  DS_TR_A("v[248:249]", tb0, 2048);
  DS_TR_A("v[250:251]", tb0, 2560);
  DS_TR_A("v[252:253]", tb1, 2048);
  DS_TR_A("v[254:255]", tb1, 2560);
  WAIT_LG(0);
  ST_R("v[240:243]", 0, rst, lane16, wto + 0 * wtk + 0 * wtn);
  MFMA_BZ(cs0, ones, "v[240:243]");
  ST_R("v[244:247]", 0, rst, lane16, wto + 0 * wtk + 1 * wtn);
  MFMA_BZ(cs1, ones, "v[244:247]");
  ST_R("v[248:251]", 0, rst, lane16, wto + 1 * wtk + 0 * wtn);
  MFMA_B(cs0, ones, "v[248:251]");
  ST_R("v[252:255]", 0, rst, lane16, wto + 1 * wtk + 1 * wtn);
  MFMA_B(cs1, ones, "v[252:255]");
  MFMA_DONE(cs1);
  PIN(cs0); PIN(cs1);
  __builtin_amdgcn_raw_buffer_store_b32(__float_as_uint(hh ? cs1[0] : cs0[0]), rst, (hh * 32 + swap23(r32)) * 4, WS_CS + unit * 256, 16);
  LD_F(fv, rws, fab_in);
  GLDS(rc, vc[2], ((9 + rot2) & 15) * 128, lw1, 114688);
  GLDS(rc, vc[3], ((9 + rot2) & 15) * 128, lw1, 118784);
  GLDS(rc, vc[1], ((10 + rot2) & 15) * 128, lw1, 122880);
  GLDS(rc, vc[2], ((10 + rot2) & 15) * 128, lw1, 126976);
  GLDS(rc, vc[3], ((10 + rot2) & 15) * 128, lw1, 131072);
  GLDS(rc, vc[1], ((11 + rot2) & 15) * 128, lw1, 135168);
  GLDS(rc, vc[2], ((11 + rot2) & 15) * 128, lw1, 139264);
  GLDS(rc, vc[3], ((11 + rot2) & 15) * 128, lw1, 143360);
  WAIT_VM(8);
  PIN(fv);
  if (!__all(fv == tag)) poll(rws, fab_in, tag);
  LD_A(0, 3, 0, rws, lane16, abo + ((0 + rot2) & 15) * 16384);
  LD_A(4, 7, 1024, rws, lane16, abo + ((0 + rot2) & 15) * 16384);
  LD_A(8, 11, 2048, rws, lane16, abo + ((0 + rot2) & 15) * 16384);
  LD_A(12, 15, 3072, rws, lane16, abo + ((0 + rot2) & 15) * 16384);
  LD_A(16, 19, 0, rws, lane16, abo + ((1 + rot2) & 15) * 16384);
  LD_A(20, 23, 1024, rws, lane16, abo + ((1 + rot2) & 15) * 16384);
  LD_A(24, 27, 2048, rws, lane16, abo + ((1 + rot2) & 15) * 16384);
  LD_A(28, 31, 3072, rws, lane16, abo + ((1 + rot2) & 15) * 16384);
  LD_A(32, 35, 0, rws, lane16, abo + ((2 + rot2) & 15) * 16384);
  LD_A(36, 39, 1024, rws, lane16, abo + ((2 + rot2) & 15) * 16384);
  LD_A(40, 43, 2048, rws, lane16, abo + ((2 + rot2) & 15) * 16384);
  LD_A(44, 47, 3072, rws, lane16, abo + ((2 + rot2) & 15) * 16384);
  WAIT_VM(12);
  BARRIER();
  DS_RD(bf[0], fb0[0], 0);
  DS_RD(bf[1], fb0[1], 0);
  DS_RD(bf[2], fb0[2], 0);
  DS_RD(bf[3], fb0[3], 0);
  DS_RD(bf[4], fb0[0], 4096);
  DS_RD(bf[5], fb0[1], 4096);
  const unsigned cw0 = lds0 + w * 1024 + lane16, cw1 = cw0 + 65536, cw2 = cw0 + 131072;
  WAIT_VL(11, 4);
  MFMA_AZ(acc1, "a[0:3]", bf[0]);
  DS_RD(bf[6], fb0[2], 4096);
  LD_A(48, 51, 0, rws, lane16, abo + ((3 + rot2) & 15) * 16384);
  WAIT_VM(11);
  MFMA_A(acc1, "a[4:7]", bf[1]);
  DS_RD(bf[7], fb0[3], 4096);
  WAIT_VL(10, 4);
  MFMA_A(acc1, "a[8:11]", bf[2]);
  DS_RD(bf[0], fb0[0], 8192);
  WAIT_VM(9);
  MFMA_A(acc1, "a[12:15]", bf[3]);
  DS_RD(bf[1], fb0[1], 8192);
  LD_A(52, 55, 1024, rws, lane16, abo + ((3 + rot2) & 15) * 16384);
  WAIT_LG(4);
  MFMA_AZ(acc2, "a[0:3]", bf[4]);
  DS_RD(bf[2], fb0[2], 8192);
  MFMA_A(acc2, "a[4:7]", bf[5]);
  DS_RD(bf[3], fb0[3], 8192);
  WAIT_LG(4);
  MFMA_A(acc2, "a[8:11]", bf[6]);
  DS_RD(bf[4], fb0[0], 12288);
  LD_A(56, 59, 2048, rws, lane16, abo + ((3 + rot2) & 15) * 16384);
  MFMA_A(acc2, "a[12:15]", bf[7]);
  DS_RD(bf[5], fb0[1], 12288);
  WAIT_LG(4);
  MFMA_AZ(acc3, "a[0:3]", bf[0]);
  DS_RD(bf[6], fb0[2], 12288);
  MFMA_A(acc3, "a[4:7]", bf[1]);
  DS_RD(bf[7], fb0[3], 12288);
  LD_A(60, 63, 3072, rws, lane16, abo + ((3 + rot2) & 15) * 16384);
  WAIT_LG(4);
  MFMA_A(acc3, "a[8:11]", bf[2]);
  DS_RD(bf[0], fb0[0], 16384);
  LD_RP("v[160:163]", rc, vc[1], ((12 + rot2) & 15) * 128);
  MFMA_A(acc3, "a[12:15]", bf[3]);
  DS_RD(bf[1], fb0[1], 16384);
  WAIT_VL(12, 4);
  MFMA_A(acc1, "a[16:19]", bf[4]);
  DS_RD(bf[2], fb0[2], 16384);
  LD_A(64, 67, 0, rws, lane16, abo + ((4 + rot2) & 15) * 16384);
  WAIT_VM(12);
  MFMA_A(acc1, "a[20:23]", bf[5]);
  DS_RD(bf[3], fb0[3], 16384);
  WAIT_VL(11, 4);
  MFMA_A(acc1, "a[24:27]", bf[6]);
  DS_RD(bf[4], fb0[0], 20480);
  WAIT_VM(10);
  MFMA_A(acc1, "a[28:31]", bf[7]);
  DS_RD(bf[5], fb0[1], 20480);
  LD_A(68, 71, 1024, rws, lane16, abo + ((4 + rot2) & 15) * 16384);
  WAIT_LG(4);
  MFMA_A(acc2, "a[16:19]", bf[0]);
  DS_RD(bf[6], fb0[2], 20480);
  LD_RP("v[164:167]", rc, vc[2], ((12 + rot2) & 15) * 128);
  MFMA_A(acc2, "a[20:23]", bf[1]);
  DS_RD(bf[7], fb0[3], 20480);
  WAIT_LG(4);
  MFMA_A(acc2, "a[24:27]", bf[2]);
  DS_RD(bf[0], fb0[0], 24576);
  LD_A(72, 75, 2048, rws, lane16, abo + ((4 + rot2) & 15) * 16384);
  MFMA_A(acc2, "a[28:31]", bf[3]);
  DS_RD(bf[1], fb0[1], 24576);
  WAIT_LG(4);
  MFMA_A(acc3, "a[16:19]", bf[4]);
  DS_RD(bf[2], fb0[2], 24576);
  st_flag(rws, tag, lane < 16 ? FOFF(FL_W + unit) + lane * 8 : OOB);
  MFMA_A(acc3, "a[20:23]", bf[5]);
  DS_RD(bf[3], fb0[3], 24576);
  LD_A(76, 79, 3072, rws, lane16, abo + ((4 + rot2) & 15) * 16384);
  WAIT_LG(4);
  MFMA_A(acc3, "a[24:27]", bf[6]);
  DS_RD(bf[4], fb0[0], 28672);
  LD_RP("v[168:171]", rc, vc[3], ((12 + rot2) & 15) * 128);
  MFMA_A(acc3, "a[28:31]", bf[7]);
  DS_RD(bf[5], fb0[1], 28672);
  WAIT_VL(15, 4);
  MFMA_A(acc1, "a[32:35]", bf[0]);
  DS_RD(bf[6], fb0[2], 28672);
  LD_A(80, 83, 0, rws, lane16, abo + ((5 + rot2) & 15) * 16384);
  WAIT_VM(15);
  MFMA_A(acc1, "a[36:39]", bf[1]);
  DS_RD(bf[7], fb0[3], 28672);
  WAIT_VL(14, 4);
  MFMA_A(acc1, "a[40:43]", bf[2]);
  DS_RD(bf[0], fb0[0], 32768);
  WAIT_VM(13);
  MFMA_A(acc1, "a[44:47]", bf[3]);
  DS_RD(bf[1], fb0[1], 32768);
  LD_A(84, 87, 1024, rws, lane16, abo + ((5 + rot2) & 15) * 16384);
  WAIT_LG(4);
  MFMA_A(acc2, "a[32:35]", bf[4]);
  DS_RD(bf[2], fb0[2], 32768);
  LD_RP("v[172:175]", rc, vc[1], ((13 + rot2) & 15) * 128);
  MFMA_A(acc2, "a[36:39]", bf[5]);
  DS_RD(bf[3], fb0[3], 32768);
  WAIT_LG(4);
  MFMA_A(acc2, "a[40:43]", bf[6]);
  DS_RD(bf[4], fb0[0], 36864);
  LD_A(88, 91, 2048, rws, lane16, abo + ((5 + rot2) & 15) * 16384);
  MFMA_A(acc2, "a[44:47]", bf[7]);
  DS_RD(bf[5], fb0[1], 36864);
  WAIT_LG(4);
  MFMA_A(acc3, "a[32:35]", bf[0]);
  DS_RD(bf[6], fb0[2], 36864);
  MFMA_A(acc3, "a[36:39]", bf[1]);
  DS_RD(bf[7], fb0[3], 36864);
  LD_A(92, 95, 3072, rws, lane16, abo + ((5 + rot2) & 15) * 16384);
  WAIT_LG(4);
  MFMA_A(acc3, "a[40:43]", bf[2]);
  DS_RD(bf[0], fb0[0], 40960);
  LD_RP("v[176:179]", rc, vc[2], ((13 + rot2) & 15) * 128);
  MFMA_A(acc3, "a[44:47]", bf[3]);
  DS_RD(bf[1], fb0[1], 40960);
  WAIT_VL(17, 4);
  MFMA_A(acc1, "a[48:51]", bf[4]);
  DS_RD(bf[2], fb0[2], 40960);
  LD_A(96, 99, 0, rws, lane16, abo + ((6 + rot2) & 15) * 16384);
  WAIT_VM(17);
  MFMA_A(acc1, "a[52:55]", bf[5]);
  DS_RD(bf[3], fb0[3], 40960);
  WAIT_VL(16, 4);
  MFMA_A(acc1, "a[56:59]", bf[6]);
  DS_RD(bf[4], fb0[0], 45056);
  WAIT_VM(15);
  MFMA_A(acc1, "a[60:63]", bf[7]);
  DS_RD(bf[5], fb0[1], 45056);
  LD_A(100, 103, 1024, rws, lane16, abo + ((6 + rot2) & 15) * 16384);
  WAIT_LG(4);
  MFMA_A(acc2, "a[48:51]", bf[0]);
  DS_RD(bf[6], fb0[2], 45056);
  LD_RP("v[180:183]", rc, vc[3], ((13 + rot2) & 15) * 128);
  MFMA_A(acc2, "a[52:55]", bf[1]);
  DS_RD(bf[7], fb0[3], 45056);
  WAIT_LG(4);
  MFMA_A(acc2, "a[56:59]", bf[2]);
  DS_RD(bf[0], fb0[0], 49152);
  LD_A(104, 107, 2048, rws, lane16, abo + ((6 + rot2) & 15) * 16384);
  MFMA_A(acc2, "a[60:63]", bf[3]);
  DS_RD(bf[1], fb0[1], 49152);
  BARRIER();
  WAIT_LG(4);
  MFMA_A(acc3, "a[48:51]", bf[4]);
  DS_RD(bf[2], fb0[2], 49152);
  MFMA_A(acc3, "a[52:55]", bf[5]);
  DS_RD(bf[3], fb0[3], 49152);
  LD_A(108, 111, 3072, rws, lane16, abo + ((6 + rot2) & 15) * 16384);
  WAIT_LG(4);
  MFMA_A(acc3, "a[56:59]", bf[6]);
  DS_RD(bf[4], fb0[0], 53248);
  WAIT_VM(18);
  DS_WR_R("v[160:163]", cw0, 0);
  MFMA_A(acc3, "a[60:63]", bf[7]);
  DS_RD(bf[5], fb0[1], 53248);
  LD_RP("v[184:187]", rc, vc[1], ((14 + rot2) & 15) * 128);
  WAIT_VL(18, 5);
  MFMA_A(acc1, "a[64:67]", bf[0]);
  DS_RD(bf[6], fb0[2], 53248);
  LD_A(112, 115, 0, rws, lane16, abo + ((7 + rot2) & 15) * 16384);
  WAIT_VM(18);
  MFMA_A(acc1, "a[68:71]", bf[1]);
  DS_RD(bf[7], fb0[3], 53248);
  WAIT_VL(16, 5);
  MFMA_A(acc1, "a[72:75]", bf[2]);
  DS_RD(bf[0], fb0[0], 57344);
  WAIT_VM(14);
  MFMA_A(acc1, "a[76:79]", bf[3]);
  DS_RD(bf[1], fb0[1], 57344);
  LD_A(116, 119, 1024, rws, lane16, abo + ((7 + rot2) & 15) * 16384);
  WAIT_LG(4);
  MFMA_A(acc2, "a[64:67]", bf[4]);
  DS_RD(bf[2], fb0[2], 57344);
  DS_WR_R("v[164:167]", cw0, 4096);
  MFMA_A(acc2, "a[68:71]", bf[5]);
  DS_RD(bf[3], fb0[3], 57344);
  LD_RP("v[188:191]", rc, vc[2], ((14 + rot2) & 15) * 128);
  WAIT_LG(5);
  MFMA_A(acc2, "a[72:75]", bf[6]);
  DS_RD(bf[4], fb0[0], 61440);
  LD_A(120, 123, 2048, rws, lane16, abo + ((7 + rot2) & 15) * 16384);
  MFMA_A(acc2, "a[76:79]", bf[7]);
  DS_RD(bf[5], fb0[1], 61440);
  WAIT_LG(5);
  MFMA_A(acc3, "a[64:67]", bf[0]);
  DS_RD(bf[6], fb0[2], 61440);
  MFMA_A(acc3, "a[68:71]", bf[1]);
  DS_RD(bf[7], fb0[3], 61440);
  LD_A(124, 127, 3072, rws, lane16, abo + ((7 + rot2) & 15) * 16384);
  WAIT_LG(4);
  MFMA_A(acc3, "a[72:75]", bf[2]);
  DS_RD(bf[0], cb1[0], 0);
  WAIT_VM(17);
  DS_WR_R("v[168:171]", cw0, 8192);
  MFMA_A(acc3, "a[76:79]", bf[3]);
  DS_RD(bf[1], cb1[1], 0);
  LD_RP("v[160:163]", rc, vc[3], ((14 + rot2) & 15) * 128);
  WAIT_VL(17, 5);
  MFMA_A(acc1, "a[80:83]", bf[4]);
  DS_RD(bf[2], cb1[2], 0);
  LD_A(128, 131, 0, rws, lane16, abo + ((8 + rot2) & 15) * 16384);
  WAIT_VM(17);
  MFMA_A(acc1, "a[84:87]", bf[5]);
  DS_RD(bf[3], cb1[3], 0);
  WAIT_VL(15, 5);
  MFMA_A(acc1, "a[88:91]", bf[6]);
  DS_RD(bf[4], cb1[0], 4096);
  WAIT_VM(14);
  MFMA_A(acc1, "a[92:95]", bf[7]);
  DS_RD(bf[5], cb1[1], 4096);
  LD_A(132, 135, 1024, rws, lane16, abo + ((8 + rot2) & 15) * 16384);
  WAIT_LG(4);
  MFMA_A(acc2, "a[80:83]", bf[0]);
  DS_RD(bf[6], cb1[2], 4096);
  DS_WR_R("v[172:175]", cw0, 12288);
  MFMA_A(acc2, "a[84:87]", bf[1]);
  DS_RD(bf[7], cb1[3], 4096);
  LD_RP("v[164:167]", rc, vc[1], ((15 + rot2) & 15) * 128);
  WAIT_LG(5);
  MFMA_A(acc2, "a[88:91]", bf[2]);
  DS_RD(bf[0], cb1[0], 8192);
  LD_A(136, 139, 2048, rws, lane16, abo + ((8 + rot2) & 15) * 16384);
  MFMA_A(acc2, "a[92:95]", bf[3]);
  DS_RD(bf[1], cb1[1], 8192);
  WAIT_LG(5);
  MFMA_A(acc3, "a[80:83]", bf[4]);
  DS_RD(bf[2], cb1[2], 8192);
  MFMA_A(acc3, "a[84:87]", bf[5]);
  DS_RD(bf[3], cb1[3], 8192);
  LD_A(140, 143, 3072, rws, lane16, abo + ((8 + rot2) & 15) * 16384);
  WAIT_LG(4);
  MFMA_A(acc3, "a[88:91]", bf[6]);
  DS_RD(bf[4], cb1[0], 12288);
  WAIT_VM(17);
  DS_WR_R("v[176:179]", cw0, 16384);
  MFMA_A(acc3, "a[92:95]", bf[7]);
  DS_RD(bf[5], cb1[1], 12288);
  LD_RP("v[168:171]", rc, vc[2], ((15 + rot2) & 15) * 128);
  WAIT_VL(17, 5);
  MFMA_A(acc1, "a[96:99]", bf[0]);
  DS_RD(bf[6], cb1[2], 12288);
  LD_A(144, 147, 0, rws, lane16, abo + ((9 + rot2) & 15) * 16384);
  WAIT_VM(17);
  MFMA_A(acc1, "a[100:103]", bf[1]);
  DS_RD(bf[7], cb1[3], 12288);
  WAIT_VL(15, 5);
  MFMA_A(acc1, "a[104:107]", bf[2]);
  DS_RD(bf[0], cb1[0], 16384);
  WAIT_VM(14);
  MFMA_A(acc1, "a[108:111]", bf[3]);
  DS_RD(bf[1], cb1[1], 16384);
  LD_A(148, 151, 1024, rws, lane16, abo + ((9 + rot2) & 15) * 16384);
  WAIT_LG(4);
  MFMA_A(acc2, "a[96:99]", bf[4]);
  DS_RD(bf[2], cb1[2], 16384);
  DS_WR_R("v[180:183]", cw0, 20480);
  MFMA_A(acc2, "a[100:103]", bf[5]);
  DS_RD(bf[3], cb1[3], 16384);
  LD_RP("v[172:175]", rc, vc[3], ((15 + rot2) & 15) * 128);
  WAIT_LG(5);
  MFMA_A(acc2, "a[104:107]", bf[6]);
  DS_RD(bf[4], cb1[0], 20480);
  LD_A(152, 155, 2048, rws, lane16, abo + ((9 + rot2) & 15) * 16384);
  MFMA_A(acc2, "a[108:111]", bf[7]);
  DS_RD(bf[5], cb1[1], 20480);
  WAIT_LG(5);
  MFMA_A(acc3, "a[96:99]", bf[0]);
  DS_RD(bf[6], cb1[2], 20480);
  MFMA_A(acc3, "a[100:103]", bf[1]);
  DS_RD(bf[7], cb1[3], 20480);
  LD_A(156, 159, 3072, rws, lane16, abo + ((9 + rot2) & 15) * 16384);
  WAIT_LG(4);
  MFMA_A(acc3, "a[104:107]", bf[2]);
  DS_RD(bf[0], cb1[0], 24576);
  LD_RP("v[176:179]", rc, vc[0], ((0 + rot2) & 15) * 128);
  MFMA_A(acc3, "a[108:111]", bf[3]);
  DS_RD(bf[1], cb1[1], 24576);
  WAIT_VM(18);
  DS_WR_R("v[184:187]", cw0, 24576);
  WAIT_VL(17, 5);
  MFMA_A(acc1, "a[112:115]", bf[4]);
  DS_RD(bf[2], cb1[2], 24576);
  LD_A(160, 163, 0, rws, lane16, abo + ((10 + rot2) & 15) * 16384);
  WAIT_VM(17);
  MFMA_A(acc1, "a[116:119]", bf[5]);
  DS_RD(bf[3], cb1[3], 24576);
  WAIT_VL(15, 5);
  MFMA_A(acc1, "a[120:123]", bf[6]);
  DS_RD(bf[4], cb1[0], 28672);
  WAIT_VM(14);
  MFMA_A(acc1, "a[124:127]", bf[7]);
  DS_RD(bf[5], cb1[1], 28672);
  LD_A(164, 167, 1024, rws, lane16, abo + ((10 + rot2) & 15) * 16384);
  WAIT_LG(5);
  MFMA_A(acc2, "a[112:115]", bf[0]);
  DS_RD(bf[6], cb1[2], 28672);
  LD_RP("v[180:183]", rc, vc[0], ((1 + rot2) & 15) * 128);
  MFMA_A(acc2, "a[116:119]", bf[1]);
  DS_RD(bf[7], cb1[3], 28672);
  DS_WR_R("v[188:191]", cw0, 28672);
  WAIT_LG(5);
  MFMA_A(acc2, "a[120:123]", bf[2]);
  DS_RD(bf[0], cb1[0], 32768);
  LD_A(168, 171, 2048, rws, lane16, abo + ((10 + rot2) & 15) * 16384);
  MFMA_A(acc2, "a[124:127]", bf[3]);
  DS_RD(bf[1], cb1[1], 32768);
  WAIT_LG(5);
  MFMA_A(acc3, "a[112:115]", bf[4]);
  DS_RD(bf[2], cb1[2], 32768);
  MFMA_A(acc3, "a[116:119]", bf[5]);
  DS_RD(bf[3], cb1[3], 32768);
  LD_A(172, 175, 3072, rws, lane16, abo + ((10 + rot2) & 15) * 16384);
  WAIT_LG(5);
  MFMA_A(acc3, "a[120:123]", bf[6]);
  DS_RD(bf[4], cb1[0], 36864);
  LD_RP("v[184:187]", rc, vc[0], ((2 + rot2) & 15) * 128);
  MFMA_A(acc3, "a[124:127]", bf[7]);
  DS_RD(bf[5], cb1[1], 36864);
  WAIT_VM(18);
  DS_WR_R("v[160:163]", cw0, 32768);
  WAIT_VL(17, 5);
  MFMA_A(acc1, "a[128:131]", bf[0]);
  DS_RD(bf[6], cb1[2], 36864);
  LD_A(176, 179, 0, rws, lane16, abo + ((11 + rot2) & 15) * 16384);
  WAIT_VM(17);
  MFMA_A(acc1, "a[132:135]", bf[1]);
  DS_RD(bf[7], cb1[3], 36864);
  WAIT_VL(15, 5);
  MFMA_A(acc1, "a[136:139]", bf[2]);
  DS_RD(bf[0], cb1[0], 40960);
  WAIT_VM(14);
  MFMA_A(acc1, "a[140:143]", bf[3]);
  DS_RD(bf[1], cb1[1], 40960);
  LD_A(180, 183, 1024, rws, lane16, abo + ((11 + rot2) & 15) * 16384);
  WAIT_LG(5);
  MFMA_A(acc2, "a[128:131]", bf[4]);
  DS_RD(bf[2], cb1[2], 40960);
  LD_RP("v[188:191]", rc, vc[0], ((3 + rot2) & 15) * 128);
  LD_F(fw, rws, fw_in);
  MFMA_A(acc2, "a[132:135]", bf[5]);
  DS_RD(bf[3], cb1[3], 40960);
  DS_WR_R("v[164:167]", cw0, 36864);
  WAIT_LG(5);
  MFMA_A(acc2, "a[136:139]", bf[6]);
  DS_RD(bf[4], cb1[0], 45056);
  LD_A(184, 187, 2048, rws, lane16, abo + ((11 + rot2) & 15) * 16384);
  MFMA_A(acc2, "a[140:143]", bf[7]);
  DS_RD(bf[5], cb1[1], 45056);
  WAIT_LG(5);
  MFMA_A(acc3, "a[128:131]", bf[0]);
  DS_RD(bf[6], cb1[2], 45056);
  MFMA_A(acc3, "a[132:135]", bf[1]);
  DS_RD(bf[7], cb1[3], 45056);
  LD_A(188, 191, 3072, rws, lane16, abo + ((11 + rot2) & 15) * 16384);
  WAIT_LG(5);
  MFMA_A(acc3, "a[136:139]", bf[2]);
  DS_RD(bf[0], cb1[0], 49152);
  LD_RP("v[160:163]", rc, vc[0], ((4 + rot2) & 15) * 128);
  MFMA_A(acc3, "a[140:143]", bf[3]);
  DS_RD(bf[1], cb1[1], 49152);
  WAIT_VM(19);
  DS_WR_R("v[168:171]", cw0, 40960);
  WAIT_VL(18, 5);
  MFMA_A(acc1, "a[144:147]", bf[4]);
  DS_RD(bf[2], cb1[2], 49152);
  LD_A(192, 195, 0, rws, lane16, abo + ((12 + rot2) & 15) * 16384);
  WAIT_VM(18);
  MFMA_A(acc1, "a[148:151]", bf[5]);
  DS_RD(bf[3], cb1[3], 49152);
  WAIT_VL(16, 5);
  MFMA_A(acc1, "a[152:155]", bf[6]);
  DS_RD(bf[4], cb1[0], 53248);
  WAIT_VM(15);
  MFMA_A(acc1, "a[156:159]", bf[7]);
  DS_RD(bf[5], cb1[1], 53248);
  LD_A(196, 199, 1024, rws, lane16, abo + ((12 + rot2) & 15) * 16384);
  WAIT_LG(4);
  BARRIER();
  MFMA_A(acc2, "a[144:147]", bf[0]);
  DS_RD(bf[6], cb1[2], 53248);
  LD_RP("v[164:167]", rc, vc[0], ((5 + rot2) & 15) * 128);
  MFMA_A(acc2, "a[148:151]", bf[1]);
  DS_RD(bf[7], cb1[3], 53248);
  DS_WR_R("v[172:175]", cw0, 45056);
  WAIT_LG(5);
  MFMA_A(acc2, "a[152:155]", bf[2]);
  DS_RD(bf[0], cb1[0], 57344);
  LD_A(200, 203, 2048, rws, lane16, abo + ((12 + rot2) & 15) * 16384);
  MFMA_A(acc2, "a[156:159]", bf[3]);
  DS_RD(bf[1], cb1[1], 57344);
  WAIT_LG(5);
  MFMA_A(acc3, "a[144:147]", bf[4]);
  DS_RD(bf[2], cb1[2], 57344);
  MFMA_A(acc3, "a[148:151]", bf[5]);
  DS_RD(bf[3], cb1[3], 57344);
  LD_A(204, 207, 3072, rws, lane16, abo + ((12 + rot2) & 15) * 16384);
  WAIT_LG(5);
  MFMA_A(acc3, "a[152:155]", bf[6]);
  DS_RD(bf[4], cb1[0], 61440);
  WAIT_VM(18);
  DS_WR_R("v[176:179]", cw0, 49152);
  MFMA_A(acc3, "a[156:159]", bf[7]);
  DS_RD(bf[5], cb1[1], 61440);
  LD_RP("v[168:171]", rc, vc[0], ((6 + rot2) & 15) * 128);
  WAIT_VL(18, 5);
  MFMA_A(acc1, "a[160:163]", bf[0]);
  DS_RD(bf[6], cb1[2], 61440);
  LD_A(208, 211, 0, rws, lane16, abo + ((13 + rot2) & 15) * 16384);
  WAIT_VM(18);
  MFMA_A(acc1, "a[164:167]", bf[1]);
  DS_RD(bf[7], cb1[3], 61440);
  WAIT_VL(16, 5);
  MFMA_A(acc1, "a[168:171]", bf[2]);
  DS_RD(bf[0], cb2[0], 0);
  WAIT_VM(15);
  MFMA_A(acc1, "a[172:175]", bf[3]);
  DS_RD(bf[1], cb2[1], 0);
  LD_A(212, 215, 1024, rws, lane16, abo + ((13 + rot2) & 15) * 16384);
  WAIT_LG(4);
  MFMA_A(acc2, "a[160:163]", bf[4]);
  DS_RD(bf[2], cb2[2], 0);
  DS_WR_R("v[180:183]", cw0, 53248);
  MFMA_A(acc2, "a[164:167]", bf[5]);
  DS_RD(bf[3], cb2[3], 0);
  LD_RP("v[172:175]", rc, vc[0], ((7 + rot2) & 15) * 128);
  WAIT_LG(5);
  MFMA_A(acc2, "a[168:171]", bf[6]);
  DS_RD(bf[4], cb2[0], 4096);
  LD_A(216, 219, 2048, rws, lane16, abo + ((13 + rot2) & 15) * 16384);
  MFMA_A(acc2, "a[172:175]", bf[7]);
  DS_RD(bf[5], cb2[1], 4096);
  WAIT_LG(5);
  MFMA_A(acc3, "a[160:163]", bf[0]);
  DS_RD(bf[6], cb2[2], 4096);
  MFMA_A(acc3, "a[164:167]", bf[1]);
  DS_RD(bf[7], cb2[3], 4096);
  LD_A(220, 223, 3072, rws, lane16, abo + ((13 + rot2) & 15) * 16384);
  WAIT_LG(4);
  MFMA_A(acc3, "a[168:171]", bf[2]);
  DS_RD(bf[0], cb2[0], 8192);
  WAIT_VM(18);
  DS_WR_R("v[184:187]", cw0, 57344);
  MFMA_A(acc3, "a[172:175]", bf[3]);
  DS_RD(bf[1], cb2[1], 8192);
  LD_RP("v[176:179]", rc, vc[0], ((8 + rot2) & 15) * 128);
  WAIT_VL(18, 5);
  MFMA_A(acc1, "a[176:179]", bf[4]);
  DS_RD(bf[2], cb2[2], 8192);
  LD_A(224, 227, 0, rws, lane16, abo + ((14 + rot2) & 15) * 16384);
  WAIT_VM(18);
  MFMA_A(acc1, "a[180:183]", bf[5]);
  DS_RD(bf[3], cb2[3], 8192);
  WAIT_VL(15, 5);
  MFMA_A(acc1, "a[184:187]", bf[6]);
  DS_RD(bf[4], cb2[0], 12288);
  WAIT_VM(14);
  MFMA_A(acc1, "a[188:191]", bf[7]);
  DS_RD(bf[5], cb2[1], 12288);
  LD_A(228, 231, 1024, rws, lane16, abo + ((14 + rot2) & 15) * 16384);
  WAIT_LG(4);
  MFMA_A(acc2, "a[176:179]", bf[0]);
  DS_RD(bf[6], cb2[2], 12288);
  DS_WR_R("v[188:191]", cw0, 61440);
  MFMA_A(acc2, "a[180:183]", bf[1]);
  DS_RD(bf[7], cb2[3], 12288);
  LD_RP("v[180:183]", rc, vc[0], ((9 + rot2) & 15) * 128);
  WAIT_LG(5);
  MFMA_A(acc2, "a[184:187]", bf[2]);
  DS_RD(bf[0], fb0[0], 0);
  LD_A(232, 235, 2048, rws, lane16, abo + ((14 + rot2) & 15) * 16384);
  MFMA_A(acc2, "a[188:191]", bf[3]);
  DS_RD(bf[1], fb0[1], 0);
  WAIT_LG(5);
  MFMA_A(acc3, "a[176:179]", bf[4]);
  DS_RD(bf[2], fb0[2], 0);
  PIN(fw);
  if (!__all(fw == tag)) poll(rws, fw_in, tag);
  MFMA_A(acc3, "a[180:183]", bf[5]);
  DS_RD(bf[3], fb0[3], 0);
  LD_A(236, 239, 3072, rws, lane16, abo + ((14 + rot2) & 15) * 16384);
  WAIT_LG(4);
  MFMA_A(acc3, "a[184:187]", bf[6]);
  DS_RD(bf[4], fb0[0], 4096);
  WAIT_VM(17);
  DS_WR_R("v[160:163]", cw1, 0);
  MFMA_A(acc3, "a[188:191]", bf[7]);
  DS_RD(bf[5], fb0[1], 4096);
  LD_RP("v[184:187]", rc, vc[0], ((10 + rot2) & 15) * 128);
  WAIT_VL(17, 5);
  MFMA_A(acc1, "a[192:195]", bf[0]);
  DS_RD(bf[6], fb0[2], 4096);
  LD_A(240, 243, 0, rws, lane16, abo + ((15 + rot2) & 15) * 16384);
  WAIT_VM(17);
  MFMA_A(acc1, "a[196:199]", bf[1]);
  DS_RD(bf[7], fb0[3], 4096);
  WAIT_VL(15, 5);
  MFMA_A(acc1, "a[200:203]", bf[2]);
  DS_RD(bf[0], fb0[0], 8192);
  WAIT_VM(14);
  MFMA_A(acc1, "a[204:207]", bf[3]);
  DS_RD(bf[1], fb0[1], 8192);
  LD_A(244, 247, 1024, rws, lane16, abo + ((15 + rot2) & 15) * 16384);
  WAIT_LG(4);
  MFMA_A(acc2, "a[192:195]", bf[4]);
  DS_RD(bf[2], fb0[2], 8192);
  DS_WR_R("v[164:167]", cw1, 4096);
  MFMA_A(acc2, "a[196:199]", bf[5]);
  DS_RD(bf[3], fb0[3], 8192);
  LD_RP("v[188:191]", rc, vc[0], ((11 + rot2) & 15) * 128);
  WAIT_LG(5);
  MFMA_A(acc2, "a[200:203]", bf[6]);
  DS_RD(bf[4], fb0[0], 12288);
  LD_A(248, 251, 2048, rws, lane16, abo + ((15 + rot2) & 15) * 16384);
  MFMA_A(acc2, "a[204:207]", bf[7]);
  DS_RD(bf[5], fb0[1], 12288);
  WAIT_LG(5);
  MFMA_A(acc3, "a[192:195]", bf[0]);
  DS_RD(bf[6], fb0[2], 12288);
  MFMA_A(acc3, "a[196:199]", bf[1]);
  DS_RD(bf[7], fb0[3], 12288);
  LD_A(252, 255, 3072, rws, lane16, abo + ((15 + rot2) & 15) * 16384);
  WAIT_LG(4);
  MFMA_A(acc3, "a[200:203]", bf[2]);
  DS_RD(bf[0], fb0[0], 16384);
  LD_RP("v[160:163]", rc, vc[0], ((12 + rot2) & 15) * 128);
  MFMA_A(acc3, "a[204:207]", bf[3]);
  DS_RD(bf[1], fb0[1], 16384);
  WAIT_VM(18);
  DS_WR_R("v[168:171]", cw1, 8192);
  WAIT_VL(17, 5);
  MFMA_A(acc1, "a[208:211]", bf[4]);
  DS_RD(bf[2], fb0[2], 16384);
  WAIT_VM(16);
  MFMA_A(acc1, "a[212:215]", bf[5]);
  DS_RD(bf[3], fb0[3], 16384);
  WAIT_VL(14, 5);
  MFMA_A(acc1, "a[216:219]", bf[6]);
  DS_RD(bf[4], fb0[0], 20480);
  WAIT_VM(13);
  MFMA_A(acc1, "a[220:223]", bf[7]);
  DS_RD(bf[5], fb0[1], 20480);
  WAIT_LG(5);
  MFMA_A(acc2, "a[208:211]", bf[0]);
  DS_RD(bf[6], fb0[2], 20480);
  LD_RP("v[164:167]", rc, vc[0], ((13 + rot2) & 15) * 128);
  MFMA_A(acc2, "a[212:215]", bf[1]);
  DS_RD(bf[7], fb0[3], 20480);
  DS_WR_R("v[172:175]", cw1, 12288);
  WAIT_LG(5);
  MFMA_A(acc2, "a[216:219]", bf[2]);
  DS_RD(bf[0], fb0[0], 24576);
  LD_R("v[192:195]", 0, rff, lane16, w1o + (0 ^ fx) * 32768 + (0 ^ z1) * 4096);
  MFMA_A(acc2, "a[220:223]", bf[3]);
  DS_RD(bf[1], fb0[1], 24576);
  WAIT_LG(5);
  MFMA_A(acc3, "a[208:211]", bf[4]);
  DS_RD(bf[2], fb0[2], 24576);
  LD_R("v[196:199]", 1024, rff, lane16, w1o + (0 ^ fx) * 32768 + (0 ^ z1) * 4096);
  MFMA_A(acc3, "a[212:215]", bf[5]);
  DS_RD(bf[3], fb0[3], 24576);
  WAIT_LG(5);
  MFMA_A(acc3, "a[216:219]", bf[6]);
  DS_RD(bf[4], fb0[0], 28672);
  LD_RP("v[168:171]", rc, vc[0], ((14 + rot2) & 15) * 128);
  MFMA_A(acc3, "a[220:223]", bf[7]);
  DS_RD(bf[5], fb0[1], 28672);
  WAIT_VM(16);
  DS_WR_R("v[176:179]", cw1, 16384);
  WAIT_VL(15, 5);
  MFMA_A(acc1, "a[224:227]", bf[0]);
  DS_RD(bf[6], fb0[2], 28672);
  LD_R("v[200:203]", 2048, rff, lane16, w1o + (0 ^ fx) * 32768 + (0 ^ z1) * 4096);
  WAIT_VM(15);
  MFMA_A(acc1, "a[228:231]", bf[1]);
  DS_RD(bf[7], fb0[3], 28672);
  WAIT_VL(13, 5);
  MFMA_A(acc1, "a[232:235]", bf[2]);
  DS_RD(bf[0], fb0[0], 32768);
  LD_R("v[204:207]", 3072, rff, lane16, w1o + (0 ^ fx) * 32768 + (0 ^ z1) * 4096);
  WAIT_VM(13);
  MFMA_A(acc1, "a[236:239]", bf[3]);
  DS_RD(bf[1], fb0[1], 32768);
  WAIT_LG(5);
  MFMA_A(acc2, "a[224:227]", bf[4]);
  DS_RD(bf[2], fb0[2], 32768);
  LD_RP("v[172:175]", rc, vc[0], ((15 + rot2) & 15) * 128);
  MFMA_A(acc2, "a[228:231]", bf[5]);
  DS_RD(bf[3], fb0[3], 32768);
  DS_WR_R("v[180:183]", cw1, 20480);
  WAIT_LG(5);
  MFMA_A(acc2, "a[232:235]", bf[6]);
  DS_RD(bf[4], fb0[0], 36864);
  LD_R("v[208:211]", 0, rff, lane16, w1o + (0 ^ fx) * 32768 + (1 ^ z1) * 4096);
  MFMA_A(acc2, "a[236:239]", bf[7]);
  DS_RD(bf[5], fb0[1], 36864);
  WAIT_LG(5);
  MFMA_A(acc3, "a[224:227]", bf[0]);
  DS_RD(bf[6], fb0[2], 36864);
  LD_R("v[212:215]", 1024, rff, lane16, w1o + (0 ^ fx) * 32768 + (1 ^ z1) * 4096);
  MFMA_A(acc3, "a[228:231]", bf[1]);
  DS_RD(bf[7], fb0[3], 36864);
  WAIT_LG(5);
  MFMA_A(acc3, "a[232:235]", bf[2]);
  DS_RD(bf[0], fb0[0], 40960);
  LD_V(cp[0], 0, rff, lane16, WS_CS + (kmc * 4 + w) * 4096);
  MFMA_A(acc3, "a[236:239]", bf[3]);
  DS_RD(bf[1], fb0[1], 40960);
  WAIT_VM(16);
  DS_WR_R("v[184:187]", cw1, 24576);
  WAIT_VL(15, 7);
  BARRIER();
  WAIT_LG(5);
  MFMA_A(acc1, "a[240:243]", bf[4]);
  DS_RD(bf[2], fb0[2], 40960);
  LD_V(cp[1], 1024, rff, lane16, WS_CS + (kmc * 4 + w) * 4096);
  WAIT_VM(15);
  MFMA_A(acc1, "a[244:247]", bf[5]);
  DS_RD(bf[3], fb0[3], 40960);
  WAIT_VL(13, 5);
  MFMA_A(acc1, "a[248:251]", bf[6]);
  DS_RD(bf[4], fb0[0], 45056);
  LD_V(cp[2], 2048, rff, lane16, WS_CS + (kmc * 4 + w) * 4096);
  WAIT_VM(13);
  MFMA_A(acc1, "a[252:255]", bf[7]);
  DS_RD(bf[5], fb0[1], 45056);
  WAIT_LG(5);
  MFMA_A(acc2, "a[240:243]", bf[0]);
  DS_RD(bf[6], fb0[2], 45056);
  LD_V(cp[3], 3072, rff, lane16, WS_CS + (kmc * 4 + w) * 4096);
  MFMA_A(acc2, "a[244:247]", bf[1]);
  DS_RD(bf[7], fb0[3], 45056);
  DS_WR_R("v[188:191]", cw1, 28672);
  WAIT_LG(5);
  MFMA_A(acc2, "a[248:251]", bf[2]);
  DS_RD(bf[0], fb0[0], 49152);
  PIN(acc1); pk0 = pack_lo(acc1); PIN(pk0);
  LD_R("v[216:219]", 2048, rff, lane16, w1o + (0 ^ fx) * 32768 + (1 ^ z1) * 4096);
  MFMA_A(acc2, "a[252:255]", bf[3]);
  DS_RD(bf[1], fb0[1], 49152);
  st_sc1(rws, pk0, lane16 + 0, exb + (((q ^ 1) * 4 + 1) * 4 + w) * 2048);
  WAIT_LG(5);
  MFMA_A(acc3, "a[240:243]", bf[4]);
  DS_RD(bf[2], fb0[2], 49152);
  pk1 = pack_hi(acc1); PIN(pk1);
  MFMA_A(acc3, "a[244:247]", bf[5]);
  DS_RD(bf[3], fb0[3], 49152);
  st_sc1(rws, pk1, lane16 + 1024, exb + (((q ^ 1) * 4 + 1) * 4 + w) * 2048);
  WAIT_LG(5);
  MFMA_A(acc3, "a[248:251]", bf[6]);
  DS_RD(bf[4], fb0[0], 53248);
  WAIT_VM(16);
  DS_WR_R("v[160:163]", cw1, 32768);
  PIN(acc2); pk0 = pack_lo(acc2); PIN(pk0);
  MFMA_A(acc3, "a[252:255]", bf[7]);
  DS_RD(bf[5], fb0[1], 53248);
  st_sc1(rws, pk0, lane16 + 0, exb + (((q ^ 2) * 4 + 2) * 4 + w) * 2048);
  WAIT_LG(5);
  MFMA_AZ(acc0, "a[0:3]", bf[0]);
  DS_RD(bf[6], fb0[2], 53248);
  pk1 = pack_hi(acc2); PIN(pk1);
  MFMA_A(acc0, "a[4:7]", bf[1]);
  DS_RD(bf[7], fb0[3], 53248);
  st_sc1(rws, pk1, lane16 + 1024, exb + (((q ^ 2) * 4 + 2) * 4 + w) * 2048);
  WAIT_LG(5);
  MFMA_A(acc0, "a[8:11]", bf[2]);
  DS_RD(bf[0], fb0[0], 57344);
  PIN(acc3); pk0 = pack_lo(acc3); PIN(pk0);
  MFMA_A(acc0, "a[12:15]", bf[3]);
  DS_RD(bf[1], fb0[1], 57344);
  st_sc1(rws, pk0, lane16 + 0, exb + (((q ^ 3) * 4 + 3) * 4 + w) * 2048);
  WAIT_LG(4);
  MFMA_A(acc0, "a[16:19]", bf[4]);
  DS_RD(bf[2], fb0[2], 57344);
  WAIT_VM(18);
  DS_WR_R("v[164:167]", cw1, 36864);
  pk1 = pack_hi(acc3); PIN(pk1);
  MFMA_A(acc0, "a[20:23]", bf[5]);
  DS_RD(bf[3], fb0[3], 57344);
  st_sc1(rws, pk1, lane16 + 1024, exb + (((q ^ 3) * 4 + 3) * 4 + w) * 2048);
  WAIT_LG(5);
  MFMA_A(acc0, "a[24:27]", bf[6]);
  DS_RD(bf[4], fb0[0], 61440);
  MFMA_A(acc0, "a[28:31]", bf[7]);
  DS_RD(bf[5], fb0[1], 61440);
  LD_R("v[220:223]", 3072, rff, lane16, w1o + (0 ^ fx) * 32768 + (1 ^ z1) * 4096);
  WAIT_LG(5);
  MFMA_A(acc0, "a[32:35]", bf[0]);
  DS_RD(bf[6], fb0[2], 61440);
  MFMA_A(acc0, "a[36:39]", bf[1]);
  DS_RD(bf[7], fb0[3], 61440);
  LD_R("v[224:227]", 0, rff, lane16, w1o + (0 ^ fx) * 32768 + (2 ^ z1) * 4096);
  WAIT_LG(4);
  MFMA_A(acc0, "a[40:43]", bf[2]);
  DS_RD(bf[0], cb1[0], 0);
  WAIT_VM(18);
  DS_WR_R("v[168:171]", cw1, 40960);
  MFMA_A(acc0, "a[44:47]", bf[3]);
  DS_RD(bf[1], cb1[1], 0);
  LD_R("v[228:231]", 1024, rff, lane16, w1o + (0 ^ fx) * 32768 + (2 ^ z1) * 4096);
  WAIT_LG(5);
  MFMA_A(acc0, "a[48:51]", bf[4]);
  DS_RD(bf[2], cb1[2], 0);
  MFMA_A(acc0, "a[52:55]", bf[5]);
  DS_RD(bf[3], cb1[3], 0);
  LD_R("v[232:235]", 2048, rff, lane16, w1o + (0 ^ fx) * 32768 + (2 ^ z1) * 4096);
  WAIT_LG(5);
  MFMA_A(acc0, "a[56:59]", bf[6]);
  DS_RD(bf[4], cb1[0], 4096);
  MFMA_A(acc0, "a[60:63]", bf[7]);
  DS_RD(bf[5], cb1[1], 4096);
  LD_R("v[236:239]", 3072, rff, lane16, w1o + (0 ^ fx) * 32768 + (2 ^ z1) * 4096);
  WAIT_LG(4);
  MFMA_A(acc0, "a[64:67]", bf[0]);
  DS_RD(bf[6], cb1[2], 4096);
  WAIT_VM(18);
  DS_WR_R("v[172:175]", cw1, 45056);
  MFMA_A(acc0, "a[68:71]", bf[1]);
  DS_RD(bf[7], cb1[3], 4096);
  LD_R("v[240:243]", 0, rff, lane16, w1o + (0 ^ fx) * 32768 + (3 ^ z1) * 4096);
  WAIT_LG(5);
  MFMA_A(acc0, "a[72:75]", bf[2]);
  DS_RD(bf[0], cb1[0], 8192);
  MFMA_A(acc0, "a[76:79]", bf[3]);
  DS_RD(bf[1], cb1[1], 8192);
  LD_R("v[244:247]", 1024, rff, lane16, w1o + (0 ^ fx) * 32768 + (3 ^ z1) * 4096);
  WAIT_LG(5);
  MFMA_A(acc0, "a[80:83]", bf[4]);
  DS_RD(bf[2], cb1[2], 8192);
  MFMA_A(acc0, "a[84:87]", bf[5]);
  DS_RD(bf[3], cb1[3], 8192);
  LD_R("v[248:251]", 2048, rff, lane16, w1o + (0 ^ fx) * 32768 + (3 ^ z1) * 4096);
  WAIT_LG(4);
  MFMA_A(acc0, "a[88:91]", bf[6]);
  DS_RD(bf[4], cb1[0], 12288);
  MFMA_A(acc0, "a[92:95]", bf[7]);
  DS_RD(bf[5], cb1[1], 12288);
  LD_R("v[252:255]", 3072, rff, lane16, w1o + (0 ^ fx) * 32768 + (3 ^ z1) * 4096);
  WAIT_LG(4);
  MFMA_A(acc0, "a[96:99]", bf[0]);
  DS_RD(bf[6], cb1[2], 12288);
  MFMA_A(acc0, "a[100:103]", bf[1]);
  DS_RD(bf[7], cb1[3], 12288);
  WAIT_LG(4);
  MFMA_A(acc0, "a[104:107]", bf[2]);
  DS_RD(bf[0], cb1[0], 16384);
  WAIT_VM(9);
  st_flag(rws, tag, lane < 48 ? fex_out : OOB);
  MFMA_A(acc0, "a[108:111]", bf[3]);
  DS_RD(bf[1], cb1[1], 16384);
  WAIT_LG(4);
  MFMA_A(acc0, "a[112:115]", bf[4]);
  DS_RD(bf[2], cb1[2], 16384);
  LD_R("a[0:3]", 0, rff, lane16, w1o + (0 ^ fx) * 32768 + (4 ^ z1) * 4096);
  MFMA_A(acc0, "a[116:119]", bf[5]);
  DS_RD(bf[3], cb1[3], 16384);
  WAIT_LG(4);
  MFMA_A(acc0, "a[120:123]", bf[6]);
  DS_RD(bf[4], cb1[0], 20480);
  LD_R("a[4:7]", 1024, rff, lane16, w1o + (0 ^ fx) * 32768 + (4 ^ z1) * 4096);
  MFMA_A(acc0, "a[124:127]", bf[7]);
  DS_RD(bf[5], cb1[1], 20480);
  BARRIER();
  WAIT_LG(4);
  MFMA_A(acc0, "a[128:131]", bf[0]);
  DS_RD(bf[6], cb1[2], 20480);
  LD_R("a[8:11]", 2048, rff, lane16, w1o + (0 ^ fx) * 32768 + (4 ^ z1) * 4096);
  MFMA_A(acc0, "a[132:135]", bf[1]);
  DS_RD(bf[7], cb1[3], 20480);
  WAIT_LG(4);
  MFMA_A(acc0, "a[136:139]", bf[2]);
  DS_RD(bf[0], cb1[0], 24576);
  LD_R("a[12:15]", 3072, rff, lane16, w1o + (0 ^ fx) * 32768 + (4 ^ z1) * 4096);
  MFMA_A(acc0, "a[140:143]", bf[3]);
  DS_RD(bf[1], cb1[1], 24576);
  WAIT_LG(4);
  MFMA_A(acc0, "a[144:147]", bf[4]);
  DS_RD(bf[2], cb1[2], 24576);
  MFMA_A(acc0, "a[148:151]", bf[5]);
  DS_RD(bf[3], cb1[3], 24576);
  WAIT_LG(4);
  MFMA_A(acc0, "a[152:155]", bf[6]);
  DS_RD(bf[4], cb1[0], 28672);
  MFMA_A(acc0, "a[156:159]", bf[7]);
  DS_RD(bf[5], cb1[1], 28672);
  WAIT_LG(4);
  MFMA_A(acc0, "a[160:163]", bf[0]);
  DS_RD(bf[6], cb1[2], 28672);
  MFMA_A(acc0, "a[164:167]", bf[1]);
  DS_RD(bf[7], cb1[3], 28672);
  WAIT_LG(4);
  MFMA_A(acc0, "a[168:171]", bf[2]);
  DS_RD(bf[0], cb1[0], 32768);
  MFMA_A(acc0, "a[172:175]", bf[3]);
  DS_RD(bf[1], cb1[1], 32768);
  WAIT_LG(4);
  MFMA_A(acc0, "a[176:179]", bf[4]);
  DS_RD(bf[2], cb1[2], 32768);
  MFMA_A(acc0, "a[180:183]", bf[5]);
  DS_RD(bf[3], cb1[3], 32768);
  WAIT_LG(4);
  MFMA_A(acc0, "a[184:187]", bf[6]);
  DS_RD(bf[4], cb1[0], 36864);
  MFMA_A(acc0, "a[188:191]", bf[7]);
  DS_RD(bf[5], cb1[1], 36864);
  WAIT_LG(4);
  MFMA_A(acc0, "a[192:195]", bf[0]);
  DS_RD(bf[6], cb1[2], 36864);
  LD_F(fv, rws, fex_in);
  MFMA_A(acc0, "a[196:199]", bf[1]);
  DS_RD(bf[7], cb1[3], 36864);
  WAIT_LG(4);
  MFMA_A(acc0, "a[200:203]", bf[2]);
  DS_RD(bf[0], cb1[0], 40960);
  LD_R("a[16:19]", 0, rff, lane16, w1o + (0 ^ fx) * 32768 + (5 ^ z1) * 4096);
  MFMA_A(acc0, "a[204:207]", bf[3]);
  DS_RD(bf[1], cb1[1], 40960);
  WAIT_LG(4);
  MFMA_A(acc0, "a[208:211]", bf[4]);
  DS_RD(bf[2], cb1[2], 40960);
  LD_R("a[20:23]", 1024, rff, lane16, w1o + (0 ^ fx) * 32768 + (5 ^ z1) * 4096);
  MFMA_A(acc0, "a[212:215]", bf[5]);
  DS_RD(bf[3], cb1[3], 40960);
  WAIT_LG(4);
  MFMA_A(acc0, "a[216:219]", bf[6]);
  DS_RD(bf[4], cb1[0], 45056);
  LD_R("a[24:27]", 2048, rff, lane16, w1o + (0 ^ fx) * 32768 + (5 ^ z1) * 4096);
  MFMA_A(acc0, "a[220:223]", bf[7]);
  DS_RD(bf[5], cb1[1], 45056);
  WAIT_LG(4);
  MFMA_A(acc0, "a[224:227]", bf[0]);
  DS_RD(bf[6], cb1[2], 45056);
  LD_R("a[28:31]", 3072, rff, lane16, w1o + (0 ^ fx) * 32768 + (5 ^ z1) * 4096);
  MFMA_A(acc0, "a[228:231]", bf[1]);
  DS_RD(bf[7], cb1[3], 45056);
  WAIT_LG(4);
  MFMA_A(acc0, "a[232:235]", bf[2]);
  MFMA_A(acc0, "a[236:239]", bf[3]);
  WAIT_LG(2);
  MFMA_A(acc0, "a[240:243]", bf[4]);
  WAIT_VM(4);
  PIN(fv);
  if (!__all(fv == tag)) poll(rws, fex_in, tag);
  LD_V(ex[0], 0, rws, lane16, exi + 8192);
  MFMA_A(acc0, "a[244:247]", bf[5]);
  WAIT_LG(0);
  MFMA_A(acc0, "a[248:251]", bf[6]);
  LD_V(ex[1], 1024, rws, lane16, exi + 8192);
  MFMA_A(acc0, "a[252:255]", bf[7]);
  LD_V(ex[2], 0, rws, lane16, exi + 16384);
  LD_V(ex[3], 1024, rws, lane16, exi + 16384);
  LD_V(ex[4], 0, rws, lane16, exi + 24576);
  LD_V(ex[5], 1024, rws, lane16, exi + 24576);
  LD_R("a[32:35]", 0, rff, lane16, w1o + (0 ^ fx) * 32768 + (6 ^ z1) * 4096);
  LD_R("a[36:39]", 1024, rff, lane16, w1o + (0 ^ fx) * 32768 + (6 ^ z1) * 4096);
  LD_R("a[40:43]", 2048, rff, lane16, w1o + (0 ^ fx) * 32768 + (6 ^ z1) * 4096);
  LD_R("a[44:47]", 3072, rff, lane16, w1o + (0 ^ fx) * 32768 + (6 ^ z1) * 4096);
  BARRIER();
  { const int kk_ = swap23(r32 & 15) - 8 * hh;
#pragma unroll
    for (int d = 0; d < 4; ++d) { const unsigned v_ = (kk_ >> 1) == d ? ((kk_ & 1) ? 0x3f800000u : 0x3f80u) : 0u; pk0[d] = r32 < 16 ? v_ : 0u; pk1[d] = r32 < 16 ? 0u : v_; }
    asm volatile("s_nop 1" : "+v"(pk0), "+v"(pk1)); }
  MFMA_V(acc0, pk0, xr0);
  MFMA_V(acc0, pk1, xr1);
  WAIT_VM(4);
  MFMA_V(acc0, pk0, ex[0]);
  MFMA_V(acc0, pk1, ex[1]);
  MFMA_V(acc0, pk0, ex[2]);
  MFMA_V(acc0, pk1, ex[3]);
  MFMA_V(acc0, pk0, ex[4]);
  MFMA_V(acc0, pk1, ex[5]);
  MFMA_DONE(acc0);
  {
    v4u h0 = pack_lo(acc0), h1 = pack_hi(acc0);
    st_sc1(rws, h0, lane16, WS_H + hpart * 2048);
    st_sc1(rws, h1, lane16 + 1024, WS_H + hpart * 2048);
    PIN(h0); PIN(h1);
    float s = 0.f, sq = 0.f, s2 = 0.f, sq2 = 0.f;
#pragma unroll
    for (int d = 0; d < 4; ++d) { s = dot2(h0[d], ONES, s); sq = dot2(h0[d], h0[d], sq); s2 = dot2(h1[d], ONES, s2); sq2 = dot2(h1[d], h1[d], sq2); }
    auto pr = __builtin_amdgcn_permlane32_swap(__float_as_uint(s + s2), __float_as_uint(sq + sq2), false, false);
    __builtin_amdgcn_raw_buffer_store_b32(__float_as_uint(__uint_as_float(pr[0]) + __uint_as_float(pr[1])), rws, HSOFF, 16); }
  LD_R("a[48:51]", 0, rff, lane16, w1o + (0 ^ fx) * 32768 + (7 ^ z1) * 4096);
  LD_R("a[52:55]", 1024, rff, lane16, w1o + (0 ^ fx) * 32768 + (7 ^ z1) * 4096);
  LD_R("a[56:59]", 2048, rff, lane16, w1o + (0 ^ fx) * 32768 + (7 ^ z1) * 4096);
  LD_R("a[60:63]", 3072, rff, lane16, w1o + (0 ^ fx) * 32768 + (7 ^ z1) * 4096);
  LD_R("a[64:67]", 0, rff, lane16, w1o + (1 ^ fx) * 32768 + (0 ^ z1) * 4096);
  LD_R("a[68:71]", 1024, rff, lane16, w1o + (1 ^ fx) * 32768 + (0 ^ z1) * 4096);
  LD_R("a[72:75]", 2048, rff, lane16, w1o + (1 ^ fx) * 32768 + (0 ^ z1) * 4096);
  LD_R("a[76:79]", 3072, rff, lane16, w1o + (1 ^ fx) * 32768 + (0 ^ z1) * 4096);
  PIN(cp[0]);
  PIN(cp[1]);
  PIN(cp[2]);
  PIN(cp[3]);
  cy = __builtin_bit_cast(f32x4, cp[0]) + __builtin_bit_cast(f32x4, cp[1]); PIN(cy);
  cy += __builtin_bit_cast(f32x4, cp[2]); PIN(cy);
  cy += __builtin_bit_cast(f32x4, cp[3]); PIN(cy);
  { auto p0 = __builtin_amdgcn_permlane32_swap(__float_as_uint(cy[0]), __float_as_uint(cy[0]), false, false); cy[0] = __uint_as_float(p0[0]) + __uint_as_float(p0[1]); PIN(cy); }
  { auto p0 = __builtin_amdgcn_permlane16_swap(__float_as_uint(cy[0]), __float_as_uint(cy[0]), false, false); cy[0] = __uint_as_float(p0[0]) + __uint_as_float(p0[1]); PIN(cy); }
  { auto p0 = __builtin_amdgcn_permlane32_swap(__float_as_uint(cy[1]), __float_as_uint(cy[1]), false, false); cy[1] = __uint_as_float(p0[0]) + __uint_as_float(p0[1]); PIN(cy); }
  { auto p0 = __builtin_amdgcn_permlane16_swap(__float_as_uint(cy[1]), __float_as_uint(cy[1]), false, false); cy[1] = __uint_as_float(p0[0]) + __uint_as_float(p0[1]); PIN(cy); }
  { auto p0 = __builtin_amdgcn_permlane32_swap(__float_as_uint(cy[2]), __float_as_uint(cy[2]), false, false); cy[2] = __uint_as_float(p0[0]) + __uint_as_float(p0[1]); PIN(cy); }
  { auto p0 = __builtin_amdgcn_permlane16_swap(__float_as_uint(cy[2]), __float_as_uint(cy[2]), false, false); cy[2] = __uint_as_float(p0[0]) + __uint_as_float(p0[1]); PIN(cy); }
  { auto p0 = __builtin_amdgcn_permlane32_swap(__float_as_uint(cy[3]), __float_as_uint(cy[3]), false, false); cy[3] = __uint_as_float(p0[0]) + __uint_as_float(p0[1]); PIN(cy); }
  { auto p0 = __builtin_amdgcn_permlane16_swap(__float_as_uint(cy[3]), __float_as_uint(cy[3]), false, false); cy[3] = __uint_as_float(p0[0]) + __uint_as_float(p0[1]); PIN(cy); }
  *(f32x4*)(smem + 144 * 1024 + w * 4096 + (lane & 15) * 16) = cy;
  WAIT_VM(8);
  st_flag(rws, tag, lane < 16 ? FOFF(FL_H + hpart) + lane * 8 : OOB);
  LD_R("a[80:83]", 0, rff, lane16, w1o + (1 ^ fx) * 32768 + (1 ^ z1) * 4096);
  LD_R("a[84:87]", 1024, rff, lane16, w1o + (1 ^ fx) * 32768 + (1 ^ z1) * 4096);
  LD_R("a[88:91]", 2048, rff, lane16, w1o + (1 ^ fx) * 32768 + (1 ^ z1) * 4096);
  LD_R("a[92:95]", 3072, rff, lane16, w1o + (1 ^ fx) * 32768 + (1 ^ z1) * 4096);
  LD_R("a[96:99]", 0, rff, lane16, w1o + (1 ^ fx) * 32768 + (2 ^ z1) * 4096);
  LD_R("a[100:103]", 1024, rff, lane16, w1o + (1 ^ fx) * 32768 + (2 ^ z1) * 4096);
  LD_R("a[104:107]", 2048, rff, lane16, w1o + (1 ^ fx) * 32768 + (2 ^ z1) * 4096);
  LD_R("a[108:111]", 3072, rff, lane16, w1o + (1 ^ fx) * 32768 + (2 ^ z1) * 4096);
  LD_F(fw, rws, fh_in);
  LD_R("v[160:163]", 0, rff, lane16, w1o + (1 ^ fx) * 32768 + (3 ^ z1) * 4096);
  LD_R("v[164:167]", 1024, rff, lane16, w1o + (1 ^ fx) * 32768 + (3 ^ z1) * 4096);
  LD_R("v[168:171]", 2048, rff, lane16, w1o + (1 ^ fx) * 32768 + (3 ^ z1) * 4096);
  LD_R("v[172:175]", 3072, rff, lane16, w1o + (1 ^ fx) * 32768 + (3 ^ z1) * 4096);
  LD_R("v[176:179]", 0, rff, lane16, w1o + (1 ^ fx) * 32768 + (4 ^ z1) * 4096);
  LD_R("v[180:183]", 1024, rff, lane16, w1o + (1 ^ fx) * 32768 + (4 ^ z1) * 4096);
  LD_R("v[184:187]", 2048, rff, lane16, w1o + (1 ^ fx) * 32768 + (4 ^ z1) * 4096);
  LD_R("v[188:191]", 3072, rff, lane16, w1o + (1 ^ fx) * 32768 + (4 ^ z1) * 4096);
  LD_R("a[112:115]", 0, rff, lane16, w1o + (1 ^ fx) * 32768 + (5 ^ z1) * 4096);
  LD_R("a[116:119]", 1024, rff, lane16, w1o + (1 ^ fx) * 32768 + (5 ^ z1) * 4096);
  LD_R("a[120:123]", 2048, rff, lane16, w1o + (1 ^ fx) * 32768 + (5 ^ z1) * 4096);
  LD_R("a[124:127]", 3072, rff, lane16, w1o + (1 ^ fx) * 32768 + (5 ^ z1) * 4096);
  WAIT_VM(12);
  PIN(fw);
  if (!__all(fw == tag)) poll(rws, fh_in, tag);
  GLDS1(rws, lane16, WS_H + ((cl * 2 + 0) * 32 + hblk * 8 + ((0 ^ HROT) ^ (4 * (z1 & 1)))) * 1024, lw8 + (0 ^ HROT) * 1024, 0);
  GLDS1(rws, lane16, WS_H + ((cl * 2 + 0) * 32 + hblk * 8 + ((1 ^ HROT) ^ (4 * (z1 & 1)))) * 1024, lw8 + (1 ^ HROT) * 1024, 0);
  GLDS1(rws, lane16, WS_H + ((cl * 2 + 0) * 32 + hblk * 8 + ((2 ^ HROT) ^ (4 * (z1 & 1)))) * 1024, lw8 + (2 ^ HROT) * 1024, 0);
  GLDS1(rws, lane16, WS_H + ((cl * 2 + 0) * 32 + hblk * 8 + ((3 ^ HROT) ^ (4 * (z1 & 1)))) * 1024, lw8 + (3 ^ HROT) * 1024, 0);
  GLDS1(rws, lane16, WS_H + ((cl * 2 + 0) * 32 + hblk * 8 + ((4 ^ HROT) ^ (4 * (z1 & 1)))) * 1024, lw8 + (4 ^ HROT) * 1024, 0);
  GLDS1(rws, lane16, WS_H + ((cl * 2 + 0) * 32 + hblk * 8 + ((5 ^ HROT) ^ (4 * (z1 & 1)))) * 1024, lw8 + (5 ^ HROT) * 1024, 0);
  GLDS1(rws, lane16, WS_H + ((cl * 2 + 0) * 32 + hblk * 8 + ((6 ^ HROT) ^ (4 * (z1 & 1)))) * 1024, lw8 + (6 ^ HROT) * 1024, 0);
  GLDS1(rws, lane16, WS_H + ((cl * 2 + 0) * 32 + hblk * 8 + ((7 ^ HROT) ^ (4 * (z1 & 1)))) * 1024, lw8 + (7 ^ HROT) * 1024, 0);
  GLDS1(rws, lane16, WS_HS + ((cl * 2 + 0) * 4 + hblk) * 1024, lw1, 65536);
  GLDS1(rws, lane16, WS_H + ((cl * 2 + 1) * 32 + hblk * 8 + ((0 ^ HROT) ^ (4 * (z1 & 1)))) * 1024, lw8 + (0 ^ HROT) * 1024, 32768);
  GLDS1(rws, lane16, WS_H + ((cl * 2 + 1) * 32 + hblk * 8 + ((1 ^ HROT) ^ (4 * (z1 & 1)))) * 1024, lw8 + (1 ^ HROT) * 1024, 32768);
  GLDS1(rws, lane16, WS_H + ((cl * 2 + 1) * 32 + hblk * 8 + ((2 ^ HROT) ^ (4 * (z1 & 1)))) * 1024, lw8 + (2 ^ HROT) * 1024, 32768);
  GLDS1(rws, lane16, WS_H + ((cl * 2 + 1) * 32 + hblk * 8 + ((3 ^ HROT) ^ (4 * (z1 & 1)))) * 1024, lw8 + (3 ^ HROT) * 1024, 32768);
  GLDS1(rws, lane16, WS_H + ((cl * 2 + 1) * 32 + hblk * 8 + ((4 ^ HROT) ^ (4 * (z1 & 1)))) * 1024, lw8 + (4 ^ HROT) * 1024, 32768);
  GLDS1(rws, lane16, WS_H + ((cl * 2 + 1) * 32 + hblk * 8 + ((5 ^ HROT) ^ (4 * (z1 & 1)))) * 1024, lw8 + (5 ^ HROT) * 1024, 32768);
  GLDS1(rws, lane16, WS_H + ((cl * 2 + 1) * 32 + hblk * 8 + ((6 ^ HROT) ^ (4 * (z1 & 1)))) * 1024, lw8 + (6 ^ HROT) * 1024, 32768);
  GLDS1(rws, lane16, WS_H + ((cl * 2 + 1) * 32 + hblk * 8 + ((7 ^ HROT) ^ (4 * (z1 & 1)))) * 1024, lw8 + (7 ^ HROT) * 1024, 32768);
  GLDS1(rws, lane16, WS_HS + ((cl * 2 + 1) * 4 + hblk) * 1024, lw1, 69632);
  LD_R("a[128:131]", 0, rff, lane16, w1o + (1 ^ fx) * 32768 + (6 ^ z1) * 4096);
  LD_R("a[132:135]", 1024, rff, lane16, w1o + (1 ^ fx) * 32768 + (6 ^ z1) * 4096);
  LD_R("a[136:139]", 2048, rff, lane16, w1o + (1 ^ fx) * 32768 + (6 ^ z1) * 4096);
  LD_R("a[140:143]", 3072, rff, lane16, w1o + (1 ^ fx) * 32768 + (6 ^ z1) * 4096);
  LD_R("a[144:147]", 0, rff, lane16, w1o + (1 ^ fx) * 32768 + (7 ^ z1) * 4096);
  LD_R("a[148:151]", 1024, rff, lane16, w1o + (1 ^ fx) * 32768 + (7 ^ z1) * 4096);
  LD_R("a[152:155]", 2048, rff, lane16, w1o + (1 ^ fx) * 32768 + (7 ^ z1) * 4096);
  LD_R("a[156:159]", 3072, rff, lane16, w1o + (1 ^ fx) * 32768 + (7 ^ z1) * 4096);
  LD_R("a[160:163]", 0, rws, va0, w2o + (0 ^ fx) * 4096 + grp(0) * 8192);
  LD_R("a[164:167]", 0, rws, va1, w2o + (0 ^ fx) * 4096 + grp(0) * 8192);
  LD_R("a[168:171]", 2048, rws, va0, w2o + (0 ^ fx) * 4096 + grp(0) * 8192);
  LD_R("a[172:175]", 2048, rws, va1, w2o + (0 ^ fx) * 4096 + grp(0) * 8192);
  WAIT_VM(21);
  BARRIER();
  const unsigned hb = lds0 + lane16, hsa = lds0 + 65536 + HSA, csa = lds0 + 144 * 1024 + w * 4096 + hh * 32;
  float rstd0, rstd1, mn0, mn1; v4u hs[4], cq[4], ub[10]; f32x2 h2[8];

  DS_RD(bf[0], hb, 0);
  DS_RD(bf[1], hb, 1024);
  DS_RD(bf[2], hb, 2048);
  DS_RD(bf[3], hb, 3072);
  DS_RD(bf[4], hb, 4096);
  DS_RD(bf[5], hb, 5120);
  WAIT_LG(4);
  MFMA_AZ(acc0, "v[192:195]", bf[0]);
  DS_RD(bf[6], hb, 6144);
  LD_R("a[176:179]", 0, rws, va0, w2o + (0 ^ fx) * 4096 + grp(1) * 8192);
  MFMA_A(acc0, "v[196:199]", bf[1]);
  DS_RD(bf[7], hb, 7168);
  WAIT_LG(4);
  MFMA_A(acc0, "v[200:203]", bf[2]);
  DS_RD(bf[0], hb, 8192);
  DS_RD2(h2[0], hsa, 0, 1);
  DS_RD2(h2[1], hsa, 2, 3);
  DS_RD2(h2[2], hsa, 4, 5);
  DS_RD2(h2[3], hsa, 6, 7);
  LD_R("a[180:183]", 0, rws, va1, w2o + (0 ^ fx) * 4096 + grp(1) * 8192);
  MFMA_A(acc0, "v[204:207]", bf[3]);
  DS_RD(bf[1], hb, 9216);
  DS_RD2(h2[4], hsa, 8, 9);
  DS_RD2(h2[5], hsa, 10, 11);
  DS_RD2(h2[6], hsa, 12, 13);
  DS_RD2(h2[7], hsa, 14, 15);
  DS_RD(hres0, hb + ((km * 4 + w) ^ (4 * z1)) * 1024, 0);
  WAIT_LG(13);
  MFMA_A(acc0, "v[208:211]", bf[4]);
  DS_RD(bf[2], hb, 10240);
  LD_R("a[184:187]", 2048, rws, va0, w2o + (0 ^ fx) * 4096 + grp(1) * 8192);
  MFMA_A(acc0, "v[212:215]", bf[5]);
  DS_RD(bf[3], hb, 11264);
  WAIT_LG(13);
  MFMA_A(acc0, "v[216:219]", bf[6]);
  DS_RD(bf[4], hb, 12288);
  LD_R("a[188:191]", 2048, rws, va1, w2o + (0 ^ fx) * 4096 + grp(1) * 8192);
  MFMA_A(acc0, "v[220:223]", bf[7]);
  DS_RD(bf[5], hb, 13312);
  WAIT_LG(10);
  PIN(h2[0]);
  PIN(h2[1]);
  PIN(h2[2]);
  PIN(h2[3]);
  { f32x2 c = (h2[0] + h2[1]) + (h2[2] + h2[3]); s0 = c[0] + c[1]; PIN(s0); }
  WAIT_LG(9);
  MFMA_A(acc0, "v[224:227]", bf[0]);
  DS_RD(bf[6], hb, 14336);
  WAIT_LG(6);
  PIN(h2[4]);
  PIN(h2[5]);
  PIN(h2[6]);
  PIN(h2[7]);
  { f32x2 c = (h2[4] + h2[5]) + (h2[6] + h2[7]); q0 = c[0] + c[1]; PIN(q0); }
  LD_R("a[192:195]", 0, rws, va0, w2o + (0 ^ fx) * 4096 + grp(2) * 8192);
  MFMA_A(acc0, "v[228:231]", bf[1]);
  DS_RD(bf[7], hb, 15360);
  WAIT_LG(4);
  MFMA_A(acc0, "v[232:235]", bf[2]);
  DS_RD(bf[0], hb, 16384);
  { s0 += q0; auto p0 = __builtin_amdgcn_permlane32_swap(__float_as_uint(s0), __float_as_uint(s0), false, false); s0 = __uint_as_float(p0[0]); q0 = __uint_as_float(p0[1]); PIN(s0); PIN(q0); }
  LD_R("a[196:199]", 0, rws, va1, w2o + (0 ^ fx) * 4096 + grp(2) * 8192);
  MFMA_A(acc0, "v[236:239]", bf[3]);
  DS_RD(bf[1], hb, 17408);
  WAIT_LG(4);
  MFMA_A(acc0, "v[240:243]", bf[4]);
  DS_RD(bf[2], hb, 18432);
  LD_R("a[200:203]", 2048, rws, va0, w2o + (0 ^ fx) * 4096 + grp(2) * 8192);
  MFMA_A(acc0, "v[244:247]", bf[5]);
  DS_RD(bf[3], hb, 19456);
  ln_scale(s0, q0, rstd0, mn0); mn0 = -mn0; PIN(rstd0); PIN(mn0); PIN(hres0);
  WAIT_LG(4);
  MFMA_A(acc0, "v[248:251]", bf[6]);
  DS_RD(bf[4], hb, 20480);
  LD_R("a[204:207]", 2048, rws, va1, w2o + (0 ^ fx) * 4096 + grp(2) * 8192);
  MFMA_A(acc0, "v[252:255]", bf[7]);
  DS_RD(bf[5], hb, 21504);
  WAIT_LG(4);
  MFMA_A(acc0, "a[0:3]", bf[0]);
  DS_RD(bf[6], hb, 22528);
  LD_R("a[208:211]", 0, rws, va0, w2o + (0 ^ fx) * 4096 + grp(3) * 8192);
  MFMA_A(acc0, "a[4:7]", bf[1]);
  DS_RD(bf[7], hb, 23552);
  WAIT_LG(4);
  MFMA_A(acc0, "a[8:11]", bf[2]);
  DS_RD(bf[0], hb, 24576);
  LD_R("a[212:215]", 0, rws, va1, w2o + (0 ^ fx) * 4096 + grp(3) * 8192);
  MFMA_A(acc0, "a[12:15]", bf[3]);
  DS_RD(bf[1], hb, 25600);
  WAIT_LG(4);
  MFMA_A(acc0, "a[16:19]", bf[4]);
  DS_RD(bf[2], hb, 26624);
  LD_R("a[216:219]", 2048, rws, va0, w2o + (0 ^ fx) * 4096 + grp(3) * 8192);
  MFMA_A(acc0, "a[20:23]", bf[5]);
  DS_RD(bf[3], hb, 27648);
  WAIT_LG(4);
  MFMA_A(acc0, "a[24:27]", bf[6]);
  DS_RD(bf[4], hb, 28672);
  LD_R("a[220:223]", 2048, rws, va1, w2o + (0 ^ fx) * 4096 + grp(3) * 8192);
  MFMA_A(acc0, "a[28:31]", bf[7]);
  DS_RD(bf[5], hb, 29696);
  WAIT_VM(24);
  BARRIER();
  WAIT_LG(4);
  MFMA_A(acc0, "a[32:35]", bf[0]);
  DS_RD(bf[6], hb, 30720);
  DS_RD(cq[0], csa + (0 ^ fx) * 128, 0);
  DS_RD(cq[1], csa + (0 ^ fx) * 128, 16);
  DS_RD(cq[2], csa + (0 ^ fx) * 128, 64);
  DS_RD(cq[3], csa + (0 ^ fx) * 128, 80);
  LD_R("a[224:227]", 0, rws, va0, w2o + (0 ^ fx) * 4096 + grp(4) * 8192);
  MFMA_A(acc0, "a[36:39]", bf[1]);
  DS_RD(bf[7], hb, 31744);
  WAIT_LG(8);
  MFMA_A(acc0, "a[40:43]", bf[2]);
  DS_RD(bf[0], hb, 32768);
  LD_R("a[228:231]", 0, rws, va1, w2o + (0 ^ fx) * 4096 + grp(4) * 8192);
  MFMA_A(acc0, "a[44:47]", bf[3]);
  DS_RD(bf[1], hb, 33792);
  WAIT_LG(8);
  MFMA_A(acc0, "a[48:51]", bf[4]);
  DS_RD(bf[2], hb, 34816);
  LD_R("a[232:235]", 2048, rws, va0, w2o + (0 ^ fx) * 4096 + grp(4) * 8192);
  MFMA_A(acc0, "a[52:55]", bf[5]);
  DS_RD(bf[3], hb, 35840);
  WAIT_LG(4);
  MFMA_A(acc0, "a[56:59]", bf[6]);
  DS_RD(bf[4], hb, 36864);
  LD_R("a[236:239]", 2048, rws, va1, w2o + (0 ^ fx) * 4096 + grp(4) * 8192);
  MFMA_A(acc0, "a[60:63]", bf[7]);
  DS_RD(bf[5], hb, 37888);
  WAIT_LG(4);
  MFMA_AZ(acc1, "v[192:195]", bf[0]);
  DS_RD(bf[6], hb, 38912);
  MFMA_A(acc1, "v[196:199]", bf[1]);
  DS_RD(bf[7], hb, 39936);
  WAIT_LG(4);
  MFMA_A(acc1, "v[200:203]", bf[2]);
  DS_RD(bf[0], hb, 40960);
  MFMA_A(acc1, "v[204:207]", bf[3]);
  DS_RD(bf[1], hb, 41984);
  PIN(cq[0]);
  PIN(cq[1]);
  PIN(cq[2]);
  PIN(cq[3]);
  PIN(acc0);
  pk0[0] = relu_pk(cvtpk(__builtin_fmaf(mn0, __uint_as_float(cq[0][0]), acc0[0]), __builtin_fmaf(mn0, __uint_as_float(cq[0][1]), acc0[1]))); PIN(pk0);
  WAIT_LG(4);
  MFMA_A(acc1, "v[208:211]", bf[4]);
  DS_RD(bf[2], hb, 43008);
  pk0[1] = relu_pk(cvtpk(__builtin_fmaf(mn0, __uint_as_float(cq[0][2]), acc0[2]), __builtin_fmaf(mn0, __uint_as_float(cq[0][3]), acc0[3]))); PIN(pk0);
  MFMA_A(acc1, "v[212:215]", bf[5]);
  DS_RD(bf[3], hb, 44032);
  pk0[2] = relu_pk(cvtpk(__builtin_fmaf(mn0, __uint_as_float(cq[1][0]), acc0[4]), __builtin_fmaf(mn0, __uint_as_float(cq[1][1]), acc0[5]))); PIN(pk0);
  WAIT_LG(4);
  MFMA_A(acc1, "v[216:219]", bf[6]);
  DS_RD(bf[4], hb, 45056);
  pk0[3] = relu_pk(cvtpk(__builtin_fmaf(mn0, __uint_as_float(cq[1][2]), acc0[6]), __builtin_fmaf(mn0, __uint_as_float(cq[1][3]), acc0[7]))); PIN(pk0);
  MFMA_A(acc1, "v[220:223]", bf[7]);
  DS_RD(bf[5], hb, 46080);
  st_sc1(rff, pk0, lane16 + 0, WS_U + ((cl * 2 + 0) * 112 + ((kmc * 4 + w) * 2 + (0 ^ fx)) * 2) * 1024);
  WAIT_LG(4);
  MFMA_A(acc1, "v[224:227]", bf[0]);
  DS_RD(bf[6], hb, 47104);
  pk1[0] = relu_pk(cvtpk(__builtin_fmaf(mn0, __uint_as_float(cq[2][0]), acc0[8]), __builtin_fmaf(mn0, __uint_as_float(cq[2][1]), acc0[9]))); PIN(pk1);
  MFMA_A(acc1, "v[228:231]", bf[1]);
  DS_RD(bf[7], hb, 48128);
  pk1[1] = relu_pk(cvtpk(__builtin_fmaf(mn0, __uint_as_float(cq[2][2]), acc0[10]), __builtin_fmaf(mn0, __uint_as_float(cq[2][3]), acc0[11]))); PIN(pk1);
  WAIT_LG(4);
  MFMA_A(acc1, "v[232:235]", bf[2]);
  DS_RD(bf[0], hb, 49152);
  pk1[2] = relu_pk(cvtpk(__builtin_fmaf(mn0, __uint_as_float(cq[3][0]), acc0[12]), __builtin_fmaf(mn0, __uint_as_float(cq[3][1]), acc0[13]))); PIN(pk1);
  MFMA_A(acc1, "v[236:239]", bf[3]);
  DS_RD(bf[1], hb, 50176);
  pk1[3] = relu_pk(cvtpk(__builtin_fmaf(mn0, __uint_as_float(cq[3][2]), acc0[14]), __builtin_fmaf(mn0, __uint_as_float(cq[3][3]), acc0[15]))); PIN(pk1);
  WAIT_LG(4);
  MFMA_A(acc1, "v[240:243]", bf[4]);
  DS_RD(bf[2], hb, 51200);
  st_sc1(rff, pk1, lane16 + 1024, WS_U + ((cl * 2 + 0) * 112 + ((kmc * 4 + w) * 2 + (0 ^ fx)) * 2) * 1024);
  MFMA_A(acc1, "v[244:247]", bf[5]);
  DS_RD(bf[3], hb, 52224);
  WAIT_LG(4);
  MFMA_A(acc1, "v[248:251]", bf[6]);
  DS_RD(bf[4], hb, 53248);
  LD_R("a[240:243]", 0, rws, va0, w2o + (0 ^ fx) * 4096 + grp(5) * 8192);
  MFMA_A(acc1, "v[252:255]", bf[7]);
  DS_RD(bf[5], hb, 54272);
  DS_RD2(h2[0], hsa, 16, 17);
  DS_RD2(h2[1], hsa, 18, 19);
  DS_RD2(h2[2], hsa, 20, 21);
  DS_RD2(h2[3], hsa, 22, 23);
  WAIT_LG(8);
  MFMA_A(acc1, "a[0:3]", bf[0]);
  DS_RD(bf[6], hb, 55296);
  DS_RD2(h2[4], hsa, 24, 25);
  DS_RD2(h2[5], hsa, 26, 27);
  DS_RD2(h2[6], hsa, 28, 29);
  DS_RD2(h2[7], hsa, 30, 31);
  DS_RD(hres1, hb + ((km * 4 + w) ^ (4 * z1)) * 1024, 32768);
  LD_R("a[244:247]", 0, rws, va1, w2o + (0 ^ fx) * 4096 + grp(5) * 8192);
  MFMA_A(acc1, "a[4:7]", bf[1]);
  DS_RD(bf[7], hb, 56320);
  WAIT_LG(13);
  MFMA_A(acc1, "a[8:11]", bf[2]);
  DS_RD(bf[0], hb, 57344);
  LD_R("a[248:251]", 2048, rws, va0, w2o + (0 ^ fx) * 4096 + grp(5) * 8192);
  MFMA_A(acc1, "a[12:15]", bf[3]);
  DS_RD(bf[1], hb, 58368);
  WAIT_LG(13);
  MFMA_A(acc1, "a[16:19]", bf[4]);
  DS_RD(bf[2], hb, 59392);
  WAIT_LG(10);
  PIN(h2[0]);
  PIN(h2[1]);
  PIN(h2[2]);
  PIN(h2[3]);
  { f32x2 c = (h2[0] + h2[1]) + (h2[2] + h2[3]); s0 = c[0] + c[1]; PIN(s0); }
  LD_R("a[252:255]", 2048, rws, va1, w2o + (0 ^ fx) * 4096 + grp(5) * 8192);
  MFMA_A(acc1, "a[20:23]", bf[5]);
  DS_RD(bf[3], hb, 60416);
  WAIT_LG(6);
  PIN(h2[4]);
  PIN(h2[5]);
  PIN(h2[6]);
  PIN(h2[7]);
  { f32x2 c = (h2[4] + h2[5]) + (h2[6] + h2[7]); q0 = c[0] + c[1]; PIN(q0); }
  WAIT_LG(4);
  MFMA_A(acc1, "a[24:27]", bf[6]);
  DS_RD(bf[4], hb, 61440);
  LD_R("v[192:195]", 0, rws, va0, w2o + (0 ^ fx) * 4096 + grp(6) * 8192);
  MFMA_A(acc1, "a[28:31]", bf[7]);
  DS_RD(bf[5], hb, 62464);
  { s0 += q0; auto p0 = __builtin_amdgcn_permlane32_swap(__float_as_uint(s0), __float_as_uint(s0), false, false); s0 = __uint_as_float(p0[0]); q0 = __uint_as_float(p0[1]); PIN(s0); PIN(q0); }
  WAIT_LG(4);
  MFMA_A(acc1, "a[32:35]", bf[0]);
  DS_RD(bf[6], hb, 63488);
  LD_R("v[196:199]", 0, rws, va1, w2o + (0 ^ fx) * 4096 + grp(6) * 8192);
  MFMA_A(acc1, "a[36:39]", bf[1]);
  DS_RD(bf[7], hb, 64512);
  WAIT_LG(4);
  MFMA_A(acc1, "a[40:43]", bf[2]);
  DS_RD(bf[0], hb, 0);
  ln_scale(s0, q0, rstd1, mn1); mn1 = -mn1; PIN(rstd1); PIN(mn1); PIN(hres1);
  LD_R("v[200:203]", 2048, rws, va0, w2o + (0 ^ fx) * 4096 + grp(6) * 8192);
  MFMA_A(acc1, "a[44:47]", bf[3]);
  DS_RD(bf[1], hb, 1024);
  WAIT_LG(4);
  MFMA_A(acc1, "a[48:51]", bf[4]);
  DS_RD(bf[2], hb, 2048);
  WAIT_VM(7);
  st_flag(rws, tag, fu_out + 0);
  MFMA_A(acc1, "a[52:55]", bf[5]);
  DS_RD(bf[3], hb, 3072);
  WAIT_LG(4);
  MFMA_A(acc1, "a[56:59]", bf[6]);
  DS_RD(bf[4], hb, 4096);
  LD_R("v[204:207]", 2048, rws, va1, w2o + (0 ^ fx) * 4096 + grp(6) * 8192);
  MFMA_A(acc1, "a[60:63]", bf[7]);
  DS_RD(bf[5], hb, 5120);
  WAIT_LG(4);
  MFMA_AZ(acc0, "a[64:67]", bf[0]);
  DS_RD(bf[6], hb, 6144);
  LD_R("v[208:211]", 0, rws, va0, w2o + (1 ^ fx) * 4096 + grp(0) * 8192);
  MFMA_A(acc0, "a[68:71]", bf[1]);
  DS_RD(bf[7], hb, 7168);
  WAIT_LG(4);
  MFMA_A(acc0, "a[72:75]", bf[2]);
  DS_RD(bf[0], hb, 8192);
  LD_R("v[212:215]", 0, rws, va1, w2o + (1 ^ fx) * 4096 + grp(0) * 8192);
  MFMA_A(acc0, "a[76:79]", bf[3]);
  DS_RD(bf[1], hb, 9216);
  PIN(cq[0]);
  PIN(cq[1]);
  PIN(cq[2]);
  PIN(cq[3]);
  PIN(acc1);
  pk0[0] = relu_pk(cvtpk(__builtin_fmaf(mn1, __uint_as_float(cq[0][0]), acc1[0]), __builtin_fmaf(mn1, __uint_as_float(cq[0][1]), acc1[1]))); PIN(pk0);
  WAIT_LG(4);
  MFMA_A(acc0, "a[80:83]", bf[4]);
  DS_RD(bf[2], hb, 10240);
  pk0[1] = relu_pk(cvtpk(__builtin_fmaf(mn1, __uint_as_float(cq[0][2]), acc1[2]), __builtin_fmaf(mn1, __uint_as_float(cq[0][3]), acc1[3]))); PIN(pk0);
  LD_R("v[216:219]", 2048, rws, va0, w2o + (1 ^ fx) * 4096 + grp(0) * 8192);
  MFMA_A(acc0, "a[84:87]", bf[5]);
  DS_RD(bf[3], hb, 11264);
  pk0[2] = relu_pk(cvtpk(__builtin_fmaf(mn1, __uint_as_float(cq[1][0]), acc1[4]), __builtin_fmaf(mn1, __uint_as_float(cq[1][1]), acc1[5]))); PIN(pk0);
  WAIT_LG(4);
  MFMA_A(acc0, "a[88:91]", bf[6]);
  DS_RD(bf[4], hb, 12288);
  pk0[3] = relu_pk(cvtpk(__builtin_fmaf(mn1, __uint_as_float(cq[1][2]), acc1[6]), __builtin_fmaf(mn1, __uint_as_float(cq[1][3]), acc1[7]))); PIN(pk0);
  LD_R("v[220:223]", 2048, rws, va1, w2o + (1 ^ fx) * 4096 + grp(0) * 8192);
  MFMA_A(acc0, "a[92:95]", bf[7]);
  DS_RD(bf[5], hb, 13312);
  st_sc1(rff, pk0, lane16 + 0, WS_U + ((cl * 2 + 1) * 112 + ((kmc * 4 + w) * 2 + (0 ^ fx)) * 2) * 1024);
  WAIT_LG(4);
  MFMA_A(acc0, "a[96:99]", bf[0]);
  DS_RD(bf[6], hb, 14336);
  pk1[0] = relu_pk(cvtpk(__builtin_fmaf(mn1, __uint_as_float(cq[2][0]), acc1[8]), __builtin_fmaf(mn1, __uint_as_float(cq[2][1]), acc1[9]))); PIN(pk1);
  MFMA_A(acc0, "a[100:103]", bf[1]);
  DS_RD(bf[7], hb, 15360);
  pk1[1] = relu_pk(cvtpk(__builtin_fmaf(mn1, __uint_as_float(cq[2][2]), acc1[10]), __builtin_fmaf(mn1, __uint_as_float(cq[2][3]), acc1[11]))); PIN(pk1);
  WAIT_LG(4);
  MFMA_A(acc0, "a[104:107]", bf[2]);
  DS_RD(bf[0], hb, 16384);
  pk1[2] = relu_pk(cvtpk(__builtin_fmaf(mn1, __uint_as_float(cq[3][0]), acc1[12]), __builtin_fmaf(mn1, __uint_as_float(cq[3][1]), acc1[13]))); PIN(pk1);
  MFMA_A(acc0, "a[108:111]", bf[3]);
  DS_RD(bf[1], hb, 17408);
  pk1[3] = relu_pk(cvtpk(__builtin_fmaf(mn1, __uint_as_float(cq[3][2]), acc1[14]), __builtin_fmaf(mn1, __uint_as_float(cq[3][3]), acc1[15]))); PIN(pk1);
  WAIT_LG(4);
  MFMA_A(acc0, "v[160:163]", bf[4]);
  DS_RD(bf[2], hb, 18432);
  st_sc1(rff, pk1, lane16 + 1024, WS_U + ((cl * 2 + 1) * 112 + ((kmc * 4 + w) * 2 + (0 ^ fx)) * 2) * 1024);
  MFMA_A(acc0, "v[164:167]", bf[5]);
  DS_RD(bf[3], hb, 19456);
  WAIT_LG(4);
  MFMA_A(acc0, "v[168:171]", bf[6]);
  DS_RD(bf[4], hb, 20480);
  LD_R("v[224:227]", 0, rws, va0, w2o + (1 ^ fx) * 4096 + grp(1) * 8192);
  MFMA_A(acc0, "v[172:175]", bf[7]);
  DS_RD(bf[5], hb, 21504);
  WAIT_LG(4);
  MFMA_A(acc0, "v[176:179]", bf[0]);
  DS_RD(bf[6], hb, 22528);
  LD_R("v[228:231]", 0, rws, va1, w2o + (1 ^ fx) * 4096 + grp(1) * 8192);
  MFMA_A(acc0, "v[180:183]", bf[1]);
  DS_RD(bf[7], hb, 23552);
  WAIT_LG(4);
  MFMA_A(acc0, "v[184:187]", bf[2]);
  DS_RD(bf[0], hb, 24576);
  LD_R("v[232:235]", 2048, rws, va0, w2o + (1 ^ fx) * 4096 + grp(1) * 8192);
  MFMA_A(acc0, "v[188:191]", bf[3]);
  DS_RD(bf[1], hb, 25600);
  WAIT_LG(4);
  MFMA_A(acc0, "a[112:115]", bf[4]);
  DS_RD(bf[2], hb, 26624);
  LD_R("v[236:239]", 2048, rws, va1, w2o + (1 ^ fx) * 4096 + grp(1) * 8192);
  MFMA_A(acc0, "a[116:119]", bf[5]);
  DS_RD(bf[3], hb, 27648);
  WAIT_LG(4);
  MFMA_A(acc0, "a[120:123]", bf[6]);
  DS_RD(bf[4], hb, 28672);
  LD_R("v[240:243]", 0, rws, va0, w2o + (1 ^ fx) * 4096 + grp(2) * 8192);
  MFMA_A(acc0, "a[124:127]", bf[7]);
  DS_RD(bf[5], hb, 29696);
  WAIT_LG(4);
  MFMA_A(acc0, "a[128:131]", bf[0]);
  DS_RD(bf[6], hb, 30720);
  LD_R("v[244:247]", 0, rws, va1, w2o + (1 ^ fx) * 4096 + grp(2) * 8192);
  MFMA_A(acc0, "a[132:135]", bf[1]);
  DS_RD(bf[7], hb, 31744);
  WAIT_LG(4);
  MFMA_A(acc0, "a[136:139]", bf[2]);
  DS_RD(bf[0], hb, 32768);
  DS_RD(cq[0], csa + (1 ^ fx) * 128, 0);
  DS_RD(cq[1], csa + (1 ^ fx) * 128, 16);
  DS_RD(cq[2], csa + (1 ^ fx) * 128, 64);
  DS_RD(cq[3], csa + (1 ^ fx) * 128, 80);
  LD_R("v[248:251]", 2048, rws, va0, w2o + (1 ^ fx) * 4096 + grp(2) * 8192);
  MFMA_A(acc0, "a[140:143]", bf[3]);
  DS_RD(bf[1], hb, 33792);
  WAIT_LG(8);
  MFMA_A(acc0, "a[144:147]", bf[4]);
  DS_RD(bf[2], hb, 34816);
  WAIT_VM(7);
  st_flag(rws, tag, fu_out + 4096);
  MFMA_A(acc0, "a[148:151]", bf[5]);
  DS_RD(bf[3], hb, 35840);
  WAIT_LG(8);
  MFMA_A(acc0, "a[152:155]", bf[6]);
  DS_RD(bf[4], hb, 36864);
  LD_R("v[252:255]", 2048, rws, va1, w2o + (1 ^ fx) * 4096 + grp(2) * 8192);
  MFMA_A(acc0, "a[156:159]", bf[7]);
  DS_RD(bf[5], hb, 37888);
  WAIT_LG(4);
  MFMA_AZ(acc1, "a[64:67]", bf[0]);
  DS_RD(bf[6], hb, 38912);
  LD_R("a[0:3]", 0, rws, va0, w2o + (1 ^ fx) * 4096 + grp(3) * 8192);
  MFMA_A(acc1, "a[68:71]", bf[1]);
  DS_RD(bf[7], hb, 39936);
  WAIT_LG(4);
  MFMA_A(acc1, "a[72:75]", bf[2]);
  DS_RD(bf[0], hb, 40960);
  LD_R("a[4:7]", 0, rws, va1, w2o + (1 ^ fx) * 4096 + grp(3) * 8192);
  MFMA_A(acc1, "a[76:79]", bf[3]);
  DS_RD(bf[1], hb, 41984);
  PIN(cq[0]);
  PIN(cq[1]);
  PIN(cq[2]);
  PIN(cq[3]);
  PIN(acc0);
  pk0[0] = relu_pk(cvtpk(__builtin_fmaf(mn0, __uint_as_float(cq[0][0]), acc0[0]), __builtin_fmaf(mn0, __uint_as_float(cq[0][1]), acc0[1]))); PIN(pk0);
  WAIT_LG(4);
  MFMA_A(acc1, "a[80:83]", bf[4]);
  DS_RD(bf[2], hb, 43008);
  pk0[1] = relu_pk(cvtpk(__builtin_fmaf(mn0, __uint_as_float(cq[0][2]), acc0[2]), __builtin_fmaf(mn0, __uint_as_float(cq[0][3]), acc0[3]))); PIN(pk0);
  LD_F(fv, rws, fu_in + 0);
  MFMA_A(acc1, "a[84:87]", bf[5]);
  DS_RD(bf[3], hb, 44032);
  pk0[2] = relu_pk(cvtpk(__builtin_fmaf(mn0, __uint_as_float(cq[1][0]), acc0[4]), __builtin_fmaf(mn0, __uint_as_float(cq[1][1]), acc0[5]))); PIN(pk0);
  WAIT_LG(4);
  MFMA_A(acc1, "a[88:91]", bf[6]);
  DS_RD(bf[4], hb, 45056);
  pk0[3] = relu_pk(cvtpk(__builtin_fmaf(mn0, __uint_as_float(cq[1][2]), acc0[6]), __builtin_fmaf(mn0, __uint_as_float(cq[1][3]), acc0[7]))); PIN(pk0);
  LD_R("a[8:11]", 2048, rws, va0, w2o + (1 ^ fx) * 4096 + grp(3) * 8192);
  MFMA_A(acc1, "a[92:95]", bf[7]);
  DS_RD(bf[5], hb, 46080);
  st_sc1(rff, pk0, lane16 + 0, WS_U + ((cl * 2 + 0) * 112 + ((kmc * 4 + w) * 2 + (1 ^ fx)) * 2) * 1024);
  WAIT_LG(4);
  MFMA_A(acc1, "a[96:99]", bf[0]);
  DS_RD(bf[6], hb, 47104);
  pk1[0] = relu_pk(cvtpk(__builtin_fmaf(mn0, __uint_as_float(cq[2][0]), acc0[8]), __builtin_fmaf(mn0, __uint_as_float(cq[2][1]), acc0[9]))); PIN(pk1);
  MFMA_A(acc1, "a[100:103]", bf[1]);
  DS_RD(bf[7], hb, 48128);
  pk1[1] = relu_pk(cvtpk(__builtin_fmaf(mn0, __uint_as_float(cq[2][2]), acc0[10]), __builtin_fmaf(mn0, __uint_as_float(cq[2][3]), acc0[11]))); PIN(pk1);
  LD_R("a[12:15]", 2048, rws, va1, w2o + (1 ^ fx) * 4096 + grp(3) * 8192);
  WAIT_LG(4);
  MFMA_A(acc1, "a[104:107]", bf[2]);
  DS_RD(bf[0], hb, 49152);
  pk1[2] = relu_pk(cvtpk(__builtin_fmaf(mn0, __uint_as_float(cq[3][0]), acc0[12]), __builtin_fmaf(mn0, __uint_as_float(cq[3][1]), acc0[13]))); PIN(pk1);
  MFMA_A(acc1, "a[108:111]", bf[3]);
  DS_RD(bf[1], hb, 50176);
  pk1[3] = relu_pk(cvtpk(__builtin_fmaf(mn0, __uint_as_float(cq[3][2]), acc0[14]), __builtin_fmaf(mn0, __uint_as_float(cq[3][3]), acc0[15]))); PIN(pk1);
  LD_R("a[16:19]", 0, rws, va0, w2o + (1 ^ fx) * 4096 + grp(4) * 8192);
  WAIT_LG(4);
  MFMA_A(acc1, "v[160:163]", bf[4]);
  DS_RD(bf[2], hb, 51200);
  st_sc1(rff, pk1, lane16 + 1024, WS_U + ((cl * 2 + 0) * 112 + ((kmc * 4 + w) * 2 + (1 ^ fx)) * 2) * 1024);
  MFMA_A(acc1, "v[164:167]", bf[5]);
  DS_RD(bf[3], hb, 52224);
  WAIT_LG(4);
  MFMA_A(acc1, "v[168:171]", bf[6]);
  DS_RD(bf[4], hb, 53248);
  LD_R("a[20:23]", 0, rws, va1, w2o + (1 ^ fx) * 4096 + grp(4) * 8192);
  MFMA_A(acc1, "v[172:175]", bf[7]);
  DS_RD(bf[5], hb, 54272);
  WAIT_LG(4);
  MFMA_A(acc1, "v[176:179]", bf[0]);
  DS_RD(bf[6], hb, 55296);
  WAIT_VM(6);
  PIN(fv);
  if (!__all(fv == tag)) poll(rws, fu_in + 0, tag);
  LD_V(ub[0], 0, rws, lane16, WS_U + ((cl * 2 + 0) * 112 + w * 28) * 1024 + (0 ^ fx) * 2048 + grp(0) * 4096);
  MFMA_A(acc1, "v[180:183]", bf[1]);
  DS_RD(bf[7], hb, 56320);
  WAIT_LG(4);
  MFMA_A(acc1, "v[184:187]", bf[2]);
  DS_RD(bf[0], hb, 57344);
  LD_V(ub[1], 1024, rws, lane16, WS_U + ((cl * 2 + 0) * 112 + w * 28) * 1024 + (0 ^ fx) * 2048 + grp(0) * 4096);
  MFMA_A(acc1, "v[188:191]", bf[3]);
  DS_RD(bf[1], hb, 58368);
  WAIT_LG(4);
  MFMA_A(acc1, "a[112:115]", bf[4]);
  DS_RD(bf[2], hb, 59392);
  LD_V(ub[2], 0, rws, lane16, WS_U + ((cl * 2 + 0) * 112 + w * 28) * 1024 + (0 ^ fx) * 2048 + grp(1) * 4096);
  MFMA_A(acc1, "a[116:119]", bf[5]);
  DS_RD(bf[3], hb, 60416);
  WAIT_LG(4);
  MFMA_A(acc1, "a[120:123]", bf[6]);
  DS_RD(bf[4], hb, 61440);
  LD_V(ub[3], 1024, rws, lane16, WS_U + ((cl * 2 + 0) * 112 + w * 28) * 1024 + (0 ^ fx) * 2048 + grp(1) * 4096);
  MFMA_A(acc1, "a[124:127]", bf[7]);
  DS_RD(bf[5], hb, 62464);
  WAIT_LG(4);
  MFMA_A(acc1, "a[128:131]", bf[0]);
  DS_RD(bf[6], hb, 63488);
  LD_V(ub[4], 0, rws, lane16, WS_U + ((cl * 2 + 0) * 112 + w * 28) * 1024 + (0 ^ fx) * 2048 + grp(2) * 4096);
  MFMA_A(acc1, "a[132:135]", bf[1]);
  DS_RD(bf[7], hb, 64512);
  WAIT_LG(4);
  MFMA_A(acc1, "a[136:139]", bf[2]);
  LD_V(ub[5], 1024, rws, lane16, WS_U + ((cl * 2 + 0) * 112 + w * 28) * 1024 + (0 ^ fx) * 2048 + grp(2) * 4096);
  MFMA_A(acc1, "a[140:143]", bf[3]);
  WAIT_LG(2);
  MFMA_A(acc1, "a[144:147]", bf[4]);
  WAIT_VM(7);
  st_flag(rws, tag, fu_out + 8192);
  MFMA_A(acc1, "a[148:151]", bf[5]);
  WAIT_LG(0);
  MFMA_A(acc1, "a[152:155]", bf[6]);
  LD_V(ub[6], 0, rws, lane16, WS_U + ((cl * 2 + 0) * 112 + w * 28) * 1024 + (0 ^ fx) * 2048 + grp(3) * 4096);
  MFMA_A(acc1, "a[156:159]", bf[7]);
  MFMA_DONE(acc1);
  PIN(cq[0]);
  PIN(cq[1]);
  PIN(cq[2]);
  PIN(cq[3]);
  PIN(acc1);
  pk0[0] = relu_pk(cvtpk(__builtin_fmaf(mn1, __uint_as_float(cq[0][0]), acc1[0]), __builtin_fmaf(mn1, __uint_as_float(cq[0][1]), acc1[1]))); PIN(pk0);
  pk0[1] = relu_pk(cvtpk(__builtin_fmaf(mn1, __uint_as_float(cq[0][2]), acc1[2]), __builtin_fmaf(mn1, __uint_as_float(cq[0][3]), acc1[3]))); PIN(pk0);
  pk0[2] = relu_pk(cvtpk(__builtin_fmaf(mn1, __uint_as_float(cq[1][0]), acc1[4]), __builtin_fmaf(mn1, __uint_as_float(cq[1][1]), acc1[5]))); PIN(pk0);
  pk0[3] = relu_pk(cvtpk(__builtin_fmaf(mn1, __uint_as_float(cq[1][2]), acc1[6]), __builtin_fmaf(mn1, __uint_as_float(cq[1][3]), acc1[7]))); PIN(pk0);
  st_sc1(rff, pk0, lane16 + 0, WS_U + ((cl * 2 + 1) * 112 + ((kmc * 4 + w) * 2 + (1 ^ fx)) * 2) * 1024);
  pk1[0] = relu_pk(cvtpk(__builtin_fmaf(mn1, __uint_as_float(cq[2][0]), acc1[8]), __builtin_fmaf(mn1, __uint_as_float(cq[2][1]), acc1[9]))); PIN(pk1);
  pk1[1] = relu_pk(cvtpk(__builtin_fmaf(mn1, __uint_as_float(cq[2][2]), acc1[10]), __builtin_fmaf(mn1, __uint_as_float(cq[2][3]), acc1[11]))); PIN(pk1);
  pk1[2] = relu_pk(cvtpk(__builtin_fmaf(mn1, __uint_as_float(cq[3][0]), acc1[12]), __builtin_fmaf(mn1, __uint_as_float(cq[3][1]), acc1[13]))); PIN(pk1);
  pk1[3] = relu_pk(cvtpk(__builtin_fmaf(mn1, __uint_as_float(cq[3][2]), acc1[14]), __builtin_fmaf(mn1, __uint_as_float(cq[3][3]), acc1[15]))); PIN(pk1);
  st_sc1(rff, pk1, lane16 + 1024, WS_U + ((cl * 2 + 1) * 112 + ((kmc * 4 + w) * 2 + (1 ^ fx)) * 2) * 1024);
  LD_V(ub[7], 1024, rws, lane16, WS_U + ((cl * 2 + 0) * 112 + w * 28) * 1024 + (0 ^ fx) * 2048 + grp(3) * 4096);
  LD_V(ub[8], 0, rws, lane16, WS_U + ((cl * 2 + 0) * 112 + w * 28) * 1024 + (0 ^ fx) * 2048 + grp(4) * 4096);
  LD_V(ub[9], 1024, rws, lane16, WS_U + ((cl * 2 + 0) * 112 + w * 28) * 1024 + (0 ^ fx) * 2048 + grp(4) * 4096);
  WAIT_VM(12);
  MFMA_AZ(acc0, "a[160:163]", ub[0]);
  LD_F(fv, rws, fu_in + 4096);
  MFMA_AZ(acc1, "a[164:167]", ub[0]);
  LD_V(ub[0], 0, rws, lane16, WS_U + ((cl * 2 + 0) * 112 + w * 28) * 1024 + (0 ^ fx) * 2048 + grp(5) * 4096);
  WAIT_VM(13);
  MFMA_A(acc0, "a[168:171]", ub[1]);
  LD_R("a[24:27]", 2048, rws, va0, w2o + (1 ^ fx) * 4096 + grp(4) * 8192);
  MFMA_A(acc1, "a[172:175]", ub[1]);
  LD_V(ub[1], 1024, rws, lane16, WS_U + ((cl * 2 + 0) * 112 + w * 28) * 1024 + (0 ^ fx) * 2048 + grp(5) * 4096);
  WAIT_VM(14);
  MFMA_A(acc0, "a[176:179]", ub[2]);
  LD_R("a[28:31]", 2048, rws, va1, w2o + (1 ^ fx) * 4096 + grp(4) * 8192);
  MFMA_A(acc1, "a[180:183]", ub[2]);
  LD_V(ub[2], 0, rws, lane16, WS_U + ((cl * 2 + 0) * 112 + w * 28) * 1024 + (0 ^ fx) * 2048 + grp(6) * 4096);
  WAIT_VM(15);
  MFMA_A(acc0, "a[184:187]", ub[3]);
  LD_R("a[32:35]", 0, rws, va0, w2o + (1 ^ fx) * 4096 + grp(5) * 8192);
  MFMA_A(acc1, "a[188:191]", ub[3]);
  LD_V(ub[3], 1024, rws, lane16, WS_U + ((cl * 2 + 0) * 112 + w * 28) * 1024 + (0 ^ fx) * 2048 + grp(6) * 4096);
  WAIT_VM(16);
  MFMA_A(acc0, "a[192:195]", ub[4]);
  WAIT_VM(7);
  PIN(fv);
  if (!__all(fv == tag)) poll(rws, fu_in + 4096, tag);
  MFMA_A(acc1, "a[196:199]", ub[4]);
  LD_V(ub[4], 0, rws, lane16, WS_U + ((cl * 2 + 1) * 112 + w * 28) * 1024 + (0 ^ fx) * 2048 + grp(0) * 4096);
  MFMA_A(acc0, "a[200:203]", ub[5]);
  MFMA_A(acc1, "a[204:207]", ub[5]);
  LD_V(ub[5], 1024, rws, lane16, WS_U + ((cl * 2 + 1) * 112 + w * 28) * 1024 + (0 ^ fx) * 2048 + grp(0) * 4096);
  MFMA_A(acc0, "a[208:211]", ub[6]);
  st_flag(rws, tag, fu_out + 12288);
  MFMA_A(acc1, "a[212:215]", ub[6]);
  LD_V(ub[6], 0, rws, lane16, WS_U + ((cl * 2 + 1) * 112 + w * 28) * 1024 + (0 ^ fx) * 2048 + grp(1) * 4096);
  MFMA_A(acc0, "a[216:219]", ub[7]);
  MFMA_A(acc1, "a[220:223]", ub[7]);
  LD_V(ub[7], 1024, rws, lane16, WS_U + ((cl * 2 + 1) * 112 + w * 28) * 1024 + (0 ^ fx) * 2048 + grp(1) * 4096);
  MFMA_A(acc0, "a[224:227]", ub[8]);
  MFMA_A(acc1, "a[228:231]", ub[8]);
  LD_V(ub[8], 0, rws, lane16, WS_U + ((cl * 2 + 1) * 112 + w * 28) * 1024 + (0 ^ fx) * 2048 + grp(2) * 4096);
  MFMA_A(acc0, "a[232:235]", ub[9]);
  MFMA_A(acc1, "a[236:239]", ub[9]);
  LD_V(ub[9], 1024, rws, lane16, WS_U + ((cl * 2 + 1) * 112 + w * 28) * 1024 + (0 ^ fx) * 2048 + grp(2) * 4096);
  WAIT_VM(13);
  MFMA_A(acc0, "a[240:243]", ub[0]);
  MFMA_A(acc1, "a[244:247]", ub[0]);
  LD_V(ub[0], 0, rws, lane16, WS_U + ((cl * 2 + 1) * 112 + w * 28) * 1024 + (0 ^ fx) * 2048 + grp(3) * 4096);
  WAIT_VM(12);
  MFMA_A(acc0, "a[248:251]", ub[1]);
  MFMA_A(acc1, "a[252:255]", ub[1]);
  LD_V(ub[1], 1024, rws, lane16, WS_U + ((cl * 2 + 1) * 112 + w * 28) * 1024 + (0 ^ fx) * 2048 + grp(3) * 4096);
  WAIT_VM(11);
  MFMA_A(acc0, "v[192:195]", ub[2]);
  MFMA_A(acc1, "v[196:199]", ub[2]);
  LD_V(ub[2], 0, rws, lane16, WS_U + ((cl * 2 + 1) * 112 + w * 28) * 1024 + (0 ^ fx) * 2048 + grp(4) * 4096);
  WAIT_VM(10);
  MFMA_A(acc0, "v[200:203]", ub[3]);
  LD_F(fv, rws, fu_in + 8192);
  MFMA_A(acc1, "v[204:207]", ub[3]);
  LD_V(ub[3], 1024, rws, lane16, WS_U + ((cl * 2 + 1) * 112 + w * 28) * 1024 + (0 ^ fx) * 2048 + grp(4) * 4096);
  WAIT_VM(11);
  MFMA_AZ(acc2, "a[160:163]", ub[4]);
  MFMA_AZ(acc3, "a[164:167]", ub[4]);
  LD_V(ub[4], 0, rws, lane16, WS_U + ((cl * 2 + 1) * 112 + w * 28) * 1024 + (0 ^ fx) * 2048 + grp(5) * 4096);
  WAIT_VM(11);
  MFMA_A(acc2, "a[168:171]", ub[5]);
  MFMA_A(acc3, "a[172:175]", ub[5]);
  LD_V(ub[5], 1024, rws, lane16, WS_U + ((cl * 2 + 1) * 112 + w * 28) * 1024 + (0 ^ fx) * 2048 + grp(5) * 4096);
  WAIT_VM(10);
  MFMA_A(acc2, "a[176:179]", ub[6]);
  LD_R("a[36:39]", 0, rws, va1, w2o + (1 ^ fx) * 4096 + grp(5) * 8192);
  MFMA_A(acc3, "a[180:183]", ub[6]);
  LD_V(ub[6], 0, rws, lane16, WS_U + ((cl * 2 + 1) * 112 + w * 28) * 1024 + (0 ^ fx) * 2048 + grp(6) * 4096);
  WAIT_VM(11);
  MFMA_A(acc2, "a[184:187]", ub[7]);
  LD_R("a[40:43]", 2048, rws, va0, w2o + (1 ^ fx) * 4096 + grp(5) * 8192);
  MFMA_A(acc3, "a[188:191]", ub[7]);
  LD_V(ub[7], 1024, rws, lane16, WS_U + ((cl * 2 + 1) * 112 + w * 28) * 1024 + (0 ^ fx) * 2048 + grp(6) * 4096);
  WAIT_VM(12);
  MFMA_A(acc2, "a[192:195]", ub[8]);
  WAIT_VM(7);
  PIN(fv);
  if (!__all(fv == tag)) poll(rws, fu_in + 8192, tag);
  MFMA_A(acc3, "a[196:199]", ub[8]);
  LD_V(ub[8], 0, rws, lane16, WS_U + ((cl * 2 + 0) * 112 + w * 28) * 1024 + (1 ^ fx) * 2048 + grp(0) * 4096);
  MFMA_A(acc2, "a[200:203]", ub[9]);
  MFMA_A(acc3, "a[204:207]", ub[9]);
  LD_V(ub[9], 1024, rws, lane16, WS_U + ((cl * 2 + 0) * 112 + w * 28) * 1024 + (1 ^ fx) * 2048 + grp(0) * 4096);
  MFMA_A(acc2, "a[208:211]", ub[0]);
  MFMA_A(acc3, "a[212:215]", ub[0]);
  LD_V(ub[0], 0, rws, lane16, WS_U + ((cl * 2 + 0) * 112 + w * 28) * 1024 + (1 ^ fx) * 2048 + grp(1) * 4096);
  MFMA_A(acc2, "a[216:219]", ub[1]);
  MFMA_A(acc3, "a[220:223]", ub[1]);
  LD_V(ub[1], 1024, rws, lane16, WS_U + ((cl * 2 + 0) * 112 + w * 28) * 1024 + (1 ^ fx) * 2048 + grp(1) * 4096);
  MFMA_A(acc2, "a[224:227]", ub[2]);
  MFMA_A(acc3, "a[228:231]", ub[2]);
  LD_V(ub[2], 0, rws, lane16, WS_U + ((cl * 2 + 0) * 112 + w * 28) * 1024 + (1 ^ fx) * 2048 + grp(2) * 4096);
  WAIT_VM(11);
  MFMA_A(acc2, "a[232:235]", ub[3]);
  MFMA_A(acc3, "a[236:239]", ub[3]);
  LD_V(ub[3], 1024, rws, lane16, WS_U + ((cl * 2 + 0) * 112 + w * 28) * 1024 + (1 ^ fx) * 2048 + grp(2) * 4096);
  WAIT_VM(11);
  MFMA_A(acc2, "a[240:243]", ub[4]);
  MFMA_A(acc3, "a[244:247]", ub[4]);
  LD_V(ub[4], 0, rws, lane16, WS_U + ((cl * 2 + 0) * 112 + w * 28) * 1024 + (1 ^ fx) * 2048 + grp(3) * 4096);
  WAIT_VM(11);
  MFMA_A(acc2, "a[248:251]", ub[5]);
  MFMA_A(acc3, "a[252:255]", ub[5]);
  LD_V(ub[5], 1024, rws, lane16, WS_U + ((cl * 2 + 0) * 112 + w * 28) * 1024 + (1 ^ fx) * 2048 + grp(3) * 4096);
  WAIT_VM(10);
  MFMA_A(acc2, "v[192:195]", ub[6]);
  MFMA_A(acc3, "v[196:199]", ub[6]);
  LD_V(ub[6], 0, rws, lane16, WS_U + ((cl * 2 + 0) * 112 + w * 28) * 1024 + (1 ^ fx) * 2048 + grp(4) * 4096);
  WAIT_VM(9);
  MFMA_A(acc2, "v[200:203]", ub[7]);
  LD_F(fv, rws, fu_in + 12288);
  MFMA_A(acc3, "v[204:207]", ub[7]);
  LD_V(ub[7], 1024, rws, lane16, WS_U + ((cl * 2 + 0) * 112 + w * 28) * 1024 + (1 ^ fx) * 2048 + grp(4) * 4096);
  WAIT_VM(10);
  MFMA_A(acc0, "v[208:211]", ub[8]);
  MFMA_A(acc1, "v[212:215]", ub[8]);
  LD_V(ub[8], 0, rws, lane16, WS_U + ((cl * 2 + 0) * 112 + w * 28) * 1024 + (1 ^ fx) * 2048 + grp(5) * 4096);
  WAIT_VM(10);
  MFMA_A(acc0, "v[216:219]", ub[9]);
  LD_R("a[44:47]", 2048, rws, va1, w2o + (1 ^ fx) * 4096 + grp(5) * 8192);
  MFMA_A(acc1, "v[220:223]", ub[9]);
  LD_V(ub[9], 1024, rws, lane16, WS_U + ((cl * 2 + 0) * 112 + w * 28) * 1024 + (1 ^ fx) * 2048 + grp(5) * 4096);
  WAIT_VM(11);
  MFMA_A(acc0, "v[224:227]", ub[0]);
  LD_R("a[48:51]", 0, rws, va0, w2o + (1 ^ fx) * 4096 + grp(6) * 8192);
  MFMA_A(acc1, "v[228:231]", ub[0]);
  LD_V(ub[0], 0, rws, lane16, WS_U + ((cl * 2 + 0) * 112 + w * 28) * 1024 + (1 ^ fx) * 2048 + grp(6) * 4096);
  WAIT_VM(12);
  MFMA_A(acc0, "v[232:235]", ub[1]);
  LD_R("a[52:55]", 0, rws, va1, w2o + (1 ^ fx) * 4096 + grp(6) * 8192);
  MFMA_A(acc1, "v[236:239]", ub[1]);
  LD_V(ub[1], 1024, rws, lane16, WS_U + ((cl * 2 + 0) * 112 + w * 28) * 1024 + (1 ^ fx) * 2048 + grp(6) * 4096);
  WAIT_VM(13);
  MFMA_A(acc0, "v[240:243]", ub[2]);
  WAIT_VM(8);
  PIN(fv);
  if (!__all(fv == tag)) poll(rws, fu_in + 12288, tag);
  MFMA_A(acc1, "v[244:247]", ub[2]);
  LD_V(ub[2], 0, rws, lane16, WS_U + ((cl * 2 + 1) * 112 + w * 28) * 1024 + (1 ^ fx) * 2048 + grp(0) * 4096);
  MFMA_A(acc0, "v[248:251]", ub[3]);
  MFMA_A(acc1, "v[252:255]", ub[3]);
  LD_V(ub[3], 1024, rws, lane16, WS_U + ((cl * 2 + 1) * 112 + w * 28) * 1024 + (1 ^ fx) * 2048 + grp(0) * 4096);
  MFMA_A(acc0, "a[0:3]", ub[4]);
  MFMA_A(acc1, "a[4:7]", ub[4]);
  LD_V(ub[4], 0, rws, lane16, WS_U + ((cl * 2 + 1) * 112 + w * 28) * 1024 + (1 ^ fx) * 2048 + grp(1) * 4096);
  MFMA_A(acc0, "a[8:11]", ub[5]);
  MFMA_A(acc1, "a[12:15]", ub[5]);
  LD_V(ub[5], 1024, rws, lane16, WS_U + ((cl * 2 + 1) * 112 + w * 28) * 1024 + (1 ^ fx) * 2048 + grp(1) * 4096);
  MFMA_A(acc0, "a[16:19]", ub[6]);
  MFMA_A(acc1, "a[20:23]", ub[6]);
  LD_V(ub[6], 0, rws, lane16, WS_U + ((cl * 2 + 1) * 112 + w * 28) * 1024 + (1 ^ fx) * 2048 + grp(2) * 4096);
  WAIT_VM(12);
  MFMA_A(acc0, "a[24:27]", ub[7]);
  MFMA_A(acc1, "a[28:31]", ub[7]);
  LD_V(ub[7], 1024, rws, lane16, WS_U + ((cl * 2 + 1) * 112 + w * 28) * 1024 + (1 ^ fx) * 2048 + grp(2) * 4096);
  WAIT_VM(12);
  MFMA_A(acc0, "a[32:35]", ub[8]);
  MFMA_A(acc1, "a[36:39]", ub[8]);
  LD_V(ub[8], 0, rws, lane16, WS_U + ((cl * 2 + 1) * 112 + w * 28) * 1024 + (1 ^ fx) * 2048 + grp(3) * 4096);
  WAIT_VM(11);
  MFMA_A(acc0, "a[40:43]", ub[9]);
  MFMA_A(acc1, "a[44:47]", ub[9]);
  LD_V(ub[9], 1024, rws, lane16, WS_U + ((cl * 2 + 1) * 112 + w * 28) * 1024 + (1 ^ fx) * 2048 + grp(3) * 4096);
  WAIT_VM(11);
  WAIT_VM(10);
  MFMA_A(acc0, "a[48:51]", ub[0]);
  WAIT_VM(9);
  MFMA_A(acc1, "a[52:55]", ub[0]);
  LD_V(ub[0], 0, rws, lane16, WS_U + ((cl * 2 + 1) * 112 + w * 28) * 1024 + (1 ^ fx) * 2048 + grp(4) * 4096);
  LD_R("a[56:59]", 2048, rws, va0, w2o + (1 ^ fx) * 4096 + grp(6) * 8192);
  WAIT_VM(0);
  MFMA_A(acc0, "a[56:59]", ub[1]);
  LD_R("a[60:63]", 2048, rws, va1, w2o + (1 ^ fx) * 4096 + grp(6) * 8192);
  WAIT_VM(0);
  MFMA_A(acc1, "a[60:63]", ub[1]);
  LD_V(ub[1], 1024, rws, lane16, WS_U + ((cl * 2 + 1) * 112 + w * 28) * 1024 + (1 ^ fx) * 2048 + grp(4) * 4096);
  MFMA_A(acc2, "v[208:211]", ub[2]);
  MFMA_A(acc3, "v[212:215]", ub[2]);
  LD_V(ub[2], 0, rws, lane16, WS_U + ((cl * 2 + 1) * 112 + w * 28) * 1024 + (1 ^ fx) * 2048 + grp(5) * 4096);
  MFMA_A(acc2, "v[216:219]", ub[3]);
  MFMA_A(acc3, "v[220:223]", ub[3]);
  LD_V(ub[3], 1024, rws, lane16, WS_U + ((cl * 2 + 1) * 112 + w * 28) * 1024 + (1 ^ fx) * 2048 + grp(5) * 4096);
  PIN(acc0); PIN(acc1);
  { f32x4 v = {acc0[8], acc0[9], acc0[10], acc0[11]}; *(f32x4*)(smem + 81920 + (((w ^ 1) * 3 + 0) * 2 + 0) * 1024 + lane16) = v; asm volatile("" ::: "memory"); }
  MFMA_A(acc2, "v[224:227]", ub[4]);
  { f32x4 v = {acc0[12], acc0[13], acc0[14], acc0[15]}; *(f32x4*)(smem + 81920 + (((w ^ 1) * 3 + 0) * 2 + 1) * 1024 + lane16) = v; asm volatile("" ::: "memory"); }
  MFMA_A(acc3, "v[228:231]", ub[4]);
  LD_V(ub[4], 0, rws, lane16, WS_U + ((cl * 2 + 1) * 112 + w * 28) * 1024 + (1 ^ fx) * 2048 + grp(6) * 4096);
  { f32x4 v = {acc1[0], acc1[1], acc1[2], acc1[3]}; *(f32x4*)(smem + 81920 + (((w ^ 2) * 3 + 1) * 2 + 0) * 1024 + lane16) = v; asm volatile("" ::: "memory"); }
  MFMA_A(acc2, "v[232:235]", ub[5]);
  { f32x4 v = {acc1[4], acc1[5], acc1[6], acc1[7]}; *(f32x4*)(smem + 81920 + (((w ^ 2) * 3 + 1) * 2 + 1) * 1024 + lane16) = v; asm volatile("" ::: "memory"); }
  MFMA_A(acc3, "v[236:239]", ub[5]);
  LD_V(ub[5], 1024, rws, lane16, WS_U + ((cl * 2 + 1) * 112 + w * 28) * 1024 + (1 ^ fx) * 2048 + grp(6) * 4096);
  { f32x4 v = {acc1[8], acc1[9], acc1[10], acc1[11]}; *(f32x4*)(smem + 81920 + (((w ^ 3) * 3 + 2) * 2 + 0) * 1024 + lane16) = v; asm volatile("" ::: "memory"); }
  MFMA_A(acc2, "v[240:243]", ub[6]);
  { f32x4 v = {acc1[12], acc1[13], acc1[14], acc1[15]}; *(f32x4*)(smem + 81920 + (((w ^ 3) * 3 + 2) * 2 + 1) * 1024 + lane16) = v; asm volatile("" ::: "memory"); }
  MFMA_A(acc3, "v[244:247]", ub[6]);
  MFMA_A(acc2, "v[248:251]", ub[7]);
  MFMA_A(acc3, "v[252:255]", ub[7]);
  MFMA_A(acc2, "a[0:3]", ub[8]);
  MFMA_A(acc3, "a[4:7]", ub[8]);
  MFMA_A(acc2, "a[8:11]", ub[9]);
  MFMA_A(acc3, "a[12:15]", ub[9]);
  MFMA_A(acc2, "a[16:19]", ub[0]);
  MFMA_A(acc3, "a[20:23]", ub[0]);
  WAIT_VM(4);
  MFMA_A(acc2, "a[24:27]", ub[1]);
  MFMA_A(acc3, "a[28:31]", ub[1]);
  WAIT_VM(3);
  MFMA_A(acc2, "a[32:35]", ub[2]);
  MFMA_A(acc3, "a[36:39]", ub[2]);
  WAIT_VM(2);
  MFMA_A(acc2, "a[40:43]", ub[3]);
  MFMA_A(acc3, "a[44:47]", ub[3]);
  WAIT_VM(1);
  MFMA_A(acc2, "a[48:51]", ub[4]);
  MFMA_A(acc3, "a[52:55]", ub[4]);
  WAIT_VM(0);
  MFMA_A(acc2, "a[56:59]", ub[5]);
  MFMA_A(acc3, "a[60:63]", ub[5]);
  asm volatile("s_nop 15" : "+v"(acc2), "+v"(acc3));
  { f32x4 v = {acc2[8], acc2[9], acc2[10], acc2[11]}; *(f32x4*)(smem + 106496 + (((w ^ 1) * 3 + 0) * 2 + 0) * 1024 + lane16) = v; asm volatile("" ::: "memory"); }
  { f32x4 v = {acc2[12], acc2[13], acc2[14], acc2[15]}; *(f32x4*)(smem + 106496 + (((w ^ 1) * 3 + 0) * 2 + 1) * 1024 + lane16) = v; asm volatile("" ::: "memory"); }
  { f32x4 v = {acc3[0], acc3[1], acc3[2], acc3[3]}; *(f32x4*)(smem + 106496 + (((w ^ 2) * 3 + 1) * 2 + 0) * 1024 + lane16) = v; asm volatile("" ::: "memory"); }
  { f32x4 v = {acc3[4], acc3[5], acc3[6], acc3[7]}; *(f32x4*)(smem + 106496 + (((w ^ 2) * 3 + 1) * 2 + 1) * 1024 + lane16) = v; asm volatile("" ::: "memory"); }
  { f32x4 v = {acc3[8], acc3[9], acc3[10], acc3[11]}; *(f32x4*)(smem + 106496 + (((w ^ 3) * 3 + 2) * 2 + 0) * 1024 + lane16) = v; asm volatile("" ::: "memory"); }
  { f32x4 v = {acc3[12], acc3[13], acc3[14], acc3[15]}; *(f32x4*)(smem + 106496 + (((w ^ 3) * 3 + 2) * 2 + 1) * 1024 + lane16) = v; asm volatile("" ::: "memory"); }
  WAIT_LG(0);
  BARRIER();
  {
    float o[8];
#pragma unroll
    for (int e = 0; e < 8; ++e) o[e] = acc0[e];
#pragma unroll
    for (int s = 0; s < 3; ++s)
#pragma unroll
      for (int g = 0; g < 2; ++g) {
        f32x4 v = *(const f32x4*)(smem + 81920 + ((w * 3 + s) * 2 + g) * 1024 + lane16);
#pragma unroll
        for (int e = 0; e < 4; ++e) o[4 * g + e] += v[e];
      }
    v4u r;
#pragma unroll
    for (int d = 0; d < 4; ++d) r[d] = cvtpk(__builtin_fmaf(o[2 * d], rstd0, bflo(hres0[d])), __builtin_fmaf(o[2 * d + 1], rstd0, bfhi(hres0[d])));
    asm volatile("buffer_store_dwordx4 %0, %1, %2, %3 offen\n\ts_nop 1" ::"v"(r), "v"(r32 * 1024 + hh * 16), "s"(mk_rsrc(out, SEQ * DM * 2)), "s"((cl * 2 + 0) * 32768 + km * 128 + w * 32) : "memory"); }
  {
    float o[8];
#pragma unroll
    for (int e = 0; e < 8; ++e) o[e] = acc2[e];
#pragma unroll
    for (int s = 0; s < 3; ++s)
#pragma unroll
      for (int g = 0; g < 2; ++g) {
        f32x4 v = *(const f32x4*)(smem + 106496 + ((w * 3 + s) * 2 + g) * 1024 + lane16);
#pragma unroll
        for (int e = 0; e < 4; ++e) o[4 * g + e] += v[e];
      }
    v4u r;
#pragma unroll
    for (int d = 0; d < 4; ++d) r[d] = cvtpk(__builtin_fmaf(o[2 * d], rstd1, bflo(hres1[d])), __builtin_fmaf(o[2 * d + 1], rstd1, bfhi(hres1[d])));
    asm volatile("buffer_store_dwordx4 %0, %1, %2, %3 offen\n\ts_nop 1" ::"v"(r), "v"(r32 * 1024 + hh * 16), "s"(mk_rsrc(out, SEQ * DM * 2)), "s"((cl * 2 + 1) * 32768 + km * 128 + w * 32) : "memory"); }
  st_flag(rws, tag, (b == 0 && tid == 0) ? 0u : OOB);
}

extern "C" void kernel_launch(void* const* d_in, const int* in_sizes, int n_in, void* d_out, int out_size, void* d_ws, size_t ws_size,
                              hipStream_t stream) {
  (void)hipFuncSetAttribute((const void*)fnet_mega, hipFuncAttributeMaxDynamicSharedMemorySize, LDS_BYTES);
  fnet_mega<<<256, 256, LDS_BYTES, stream>>>((const u16*)d_in[0], (const u16*)d_in[1], (const u16*)d_in[2], (const u16*)d_in[3], (const u16*)d_in[4],
                                             (const u16*)d_in[6], (const u16*)d_in[7], (u16*)d_out, (char*)d_ws);
}
```
